```python
import torch

def _softmax(x, axis=-1):
    return torch.softmax(x, dim=axis)
def _gelu(x):
    return torch.nn.functional.gelu(x, approximate="tanh")
def _silu(x):
    return torch.nn.functional.silu(x)
def _sigmoid(x):
    return torch.sigmoid(x)
def _layernorm(x, axis=-1, eps=1e-5):
    m = x.mean(dim=axis, keepdim=True)
    v = x.var(dim=axis, keepdim=True, unbiased=False)
    return (x - m) / torch.sqrt(v + eps)
def _scatter_add(tgt, idx, src, axis=0):
    return tgt.clone().index_add_(axis, idx, src)
def _segment_sum(src, seg, n):
    out = torch.zeros((n,) + src.shape[1:], dtype=src.dtype)
    return out.index_add_(0, seg, src)
def _pad(x, pw):
    flat = tuple(v for pair in reversed(pw) for v in pair)
    return torch.nn.functional.pad(x, flat)

kernel_name = 'recurrent_scan__sliding_block_cached__dense_activation_relu__prompt'

def setup_inputs():

    _w = (lambda a: (lambda t: t.to(torch.bfloat16) if t.is_floating_point() else t)(torch.as_tensor(a)))
    def _A_setup():
        B, Sq, D = 1, 2048, 1024
        Hq, Hkv, Sk, Dh = 32, 4, 2048, 128
        assert Hq % Hkv == 0, (Hq, Hkv)
        return {
            "x": _w((lambda *s: torch.randn(*s, dtype=torch.bfloat16) * 0.05)(B, Sq, D)),
            "k_cache": _w((lambda *s: torch.randn(*s, dtype=torch.bfloat16) * 0.8)(B, Hkv, Sk, Dh)),
            "v_cache": _w((lambda *s: torch.randn(*s, dtype=torch.bfloat16) * 0.2)(B, Hkv, Sk, Dh)),
            "wq": _w((lambda *s: torch.randn(*s, dtype=torch.bfloat16) * 0.8)(D, Hq * Dh) * (D ** -0.5)),
            "wo": _w((lambda *s: torch.randn(*s, dtype=torch.bfloat16) * 0.2)(Hq * Dh, D) * ((Hq * Dh) ** -0.5)),
        }
    _A_in = _A_setup()

    def _B_setup():
        B, S, D, Dff = 1, 2048, 1024, 3584
        return {
            "x": _w((lambda *s: torch.randn(*s, dtype=torch.bfloat16) * 0.05)(B, S, D)),
            "w1": _w((lambda *s: torch.randn(*s, dtype=torch.bfloat16) * 0.1)(D, Dff)),
            "w2": _w((lambda *s: torch.randn(*s, dtype=torch.bfloat16) * 0.1)(Dff, D)),
        }
    _B_in = _B_setup()

    return {
        "A_x": _A_in["x"],
        "A_k_cache": _A_in["k_cache"],
        "A_v_cache": _A_in["v_cache"],
        "A_wq": _A_in["wq"],
        "A_wo": _A_in["wo"],
        "B_x": _B_in["x"],
        "B_w1": _B_in["w1"],
        "B_w2": _B_in["w2"],
    }

def reference(A_x, A_k_cache, A_v_cache, A_wq, A_wo, B_x, B_w1, B_w2):
    def _A_ref(x, k_cache, v_cache, wq, wo):
        B, Sq, D = x.shape
        Hq, Dh = 32, 128
        Hkv, Sk = k_cache.shape[1], k_cache.shape[2]
        W = min(4096, max(256, Sk // 4))
        rep = Hq // Hkv
        q = (lambda x, *d: x.permute(*d))((x @ wq).reshape(B, Sq, Hq, Dh), 0, 2, 1, 3)
        k = (lambda x, n, axis: torch.repeat_interleave(x, n, dim=axis))(k_cache[:, :, Sk - W:, :], rep, axis=1)
        v = (lambda x, n, axis: torch.repeat_interleave(x, n, dim=axis))(v_cache[:, :, Sk - W:, :], rep, axis=1)
        scores = torch.einsum("bhqd,bhkd->bhqk", q, k) * (Dh ** -0.5)
        p = _softmax(scores, axis=-1)
        o = torch.einsum("bhqk,bhkd->bhqd", p, v)
        return (lambda x, *d: x.permute(*d))(o, 0, 2, 1, 3).reshape(B, Sq, Hq * Dh) @ wo

    def _B_ref(x, w1, w2):
        h = torch.einsum("bsd,df->bsf", x, w1)
        zero = torch.zeros_like(h)
        h = torch.maximum(h, zero)
        return torch.einsum("bsf,fd->bsd", h, w2)

    x = A_x
    xn = _layernorm(x, axis=-1)
    s = _A_ref(xn, A_k_cache, A_v_cache, A_wq, A_wo)
    g = _silu(_B_ref(xn, B_w1, B_w2))
    return x + s * g
```

```cpp
#include <hip/hip_runtime.h>
#include <stdint.h>

typedef short bf16x8 __attribute__((ext_vector_type(8)));
typedef short s16x4 __attribute__((ext_vector_type(4)));
typedef float f32x16 __attribute__((ext_vector_type(16)));
typedef float f32x2 __attribute__((ext_vector_type(2)));
typedef unsigned u32x4 __attribute__((ext_vector_type(4)));
typedef unsigned short u16;
typedef __bf16 bf16x2_t __attribute__((ext_vector_type(2)));
#define LDSP __attribute__((address_space(3)))
#define DI __device__ __forceinline__

DI float bf2f(u16 v) { return __uint_as_float(((unsigned)v) << 16); }
DI unsigned pk_bf16(float lo, float hi) {
    f32x2 f = {lo, hi};
    bf16x2_t b = __builtin_convertvector(f, bf16x2_t);
    return __builtin_bit_cast(unsigned, b);
}
DI float rbf(float x) {
    unsigned p = pk_bf16(x, x);
    return __uint_as_float(p << 16);
}
DI s16x4 tr_read(const char LDSP* p) { return __builtin_amdgcn_ds_read_tr16_b64_v4i16((s16x4 LDSP*)p); }
DI bf16x8 cat8(s16x4 lo, s16x4 hi) { return __builtin_shufflevector(lo, hi, 0, 1, 2, 3, 4, 5, 6, 7); }
#define MFMA32(a, b, c) __builtin_amdgcn_mfma_f32_32x32x16_bf16((a), (b), (c), 0, 0, 0)

#include <type_traits>
template <int I, int N, class F>
DI void static_for(F&& f) {
    if constexpr (I < N) {
        f(std::integral_constant<int, I>{});
        static_for<I + 1, N>(f);
    }
}
typedef float f32x4 __attribute__((ext_vector_type(4)));
#define MFMA16(a, b, c) __builtin_amdgcn_mfma_f32_16x16x32_bf16((a), (b), (c), 0, 0, 0)

template <int BM_, int BN_, int BK_, int NWM_, int NWN_, int S_, int SH_>
struct GC {
    static constexpr int BM = BM_, BN = BN_, BK = BK_, NWM = NWM_, NWN = NWN_, S = S_, SH = SH_;
    static constexpr int NW = NWM * NWN, NT = NW * 64;
    static constexpr int TT = SH, KK = SH == 32 ? 16 : 32;
    static constexpr int TM = BM / TT / NWM, TN = BN / TT / NWN, KS = BK / KK, NTL = BN / 32, KS16 = BK / 16;
    static constexpr int XB = BM * BK * 2, WB = BN * BK * 2, SB = XB + WB;
    static constexpr int XP = XB / 1024, WP = WB / 1024, XPW = XP / NW, WPW = WP / NW, L = XPW + WPW;
    static constexpr int LDS_BYTES = S * SB;
    static constexpr int NM = TN * TM, NR = 2 * TN + TM;
    static_assert(XP % NW == 0 && WP % NW == 0, "pieces per wave");
    static_assert((S * KS) % 2 == 0, "fragment double buffer parity");
    static_assert(BN % 128 == 0, "BN");
    static_assert(SH == 32 || (TM % 2 == 0 && TN % 2 == 0), "TM/TN even for the 16x16 shape");
};
template <int SH> struct AccT;
template <> struct AccT<32> { typedef f32x16 t; };
template <> struct AccT<16> { typedef f32x4 t; };

template <class C>
struct GemmState {
    const char* xsrc[C::XPW];
    const char* wsrc[C::WPW];
    long xstride, wstride;
    char LDSP* lds;
    unsigned xdst, wdst;
    unsigned a_off[2], b_off;
};

template <int N> DI void wait_vm_lgkm0() { asm volatile("s_waitcnt vmcnt(%0) lgkmcnt(0)" ::"n"(N) : "memory"); }
DI void wg_barrier() {
    asm volatile("" ::: "memory");
    __builtin_amdgcn_s_barrier();
    asm volatile("" ::: "memory");
}

template <class C>
struct GemmFrags {
    s16x4 alo[2][C::TN], ahi[2][C::TN];
    bf16x8 b[2][C::TM];
};
template <class C>
DI void gemm_read_op(const GemmState<C>& st, int k, int buf, int ks, int sel, GemmFrags<C>& f) {
    if (k >= 2 && k < 2 + C::TM) {
        const int tb = k - 2;
        if constexpr (C::SH == 32)
            f.b[sel][tb] = *(const bf16x8 LDSP*)(st.lds + (buf * C::SB + (tb * C::KS16 + ks) * 1024) + st.b_off);
        else
            f.b[sel][tb] = *(const bf16x8 LDSP*)(st.lds + (buf * C::SB + ((tb >> 1) * C::KS16 + 2 * ks) * 1024 + (tb & 1) * 256) + st.b_off);
    } else {
        const int idx = k < 2 ? k : k - C::TM, ta = idx >> 1, half = idx & 1;
        const char LDSP* p;
        if constexpr (C::SH == 32) p = st.lds + (buf * C::SB + C::XB + ((4 * ks + half) * C::NTL + ta) * 256) + st.a_off[0];
        else p = st.lds + (buf * C::SB + C::XB + ((8 * ks + half) * C::NTL + (ta >> 1)) * 256) + st.a_off[ta & 1];
        if (half) f.ahi[sel][ta] = tr_read(p); else f.alo[sel][ta] = tr_read(p);
    }
}
template <class C>
DI void gemm_glds_op(const GemmState<C>& st, int g, int t, int buf) {
    if (g < C::XPW)
        __builtin_amdgcn_global_load_lds((const unsigned*)(st.xsrc[g] + (long)t * st.xstride),
                                         (unsigned LDSP*)(st.lds + buf * C::SB + st.xdst + g * 1024), 16, 0, 0);
    else
        __builtin_amdgcn_global_load_lds((const unsigned*)(st.wsrc[g - C::XPW] + (long)t * st.wstride),
                                         (unsigned LDSP*)(st.lds + buf * C::SB + C::XB + st.wdst + (g - C::XPW) * 1024), 16, 0, 0);
}
template <class C> constexpr int read_slot(int k) {
    constexpr int SPAN = C::NM - (C::NM / 4 > 1 ? C::NM / 4 : 1);
    return k * SPAN / C::NR;
}
template <class C> constexpr int glds_slot(int g) {
    constexpr int OFF = C::NM / 4;
    return OFF + g * (C::NM - OFF) / C::L;
}

template <class C, int B, int KSI, bool TAIL>
DI void gemm_step(const GemmState<C>& st, int t, typename AccT<C::SH>::t (&acc)[C::TN][C::TM], GemmFrags<C>& f) {
    constexpr int cur = (B * C::KS + KSI) & 1, nxt = cur ^ 1;
    constexpr bool LAST = KSI == C::KS - 1;
    constexpr bool HAS_NEXT = !(LAST && TAIL && B == C::S - 1);
    constexpr bool SYNC = LAST && HAS_NEXT, ISSUE = SYNC && !TAIL;
    constexpr int nbuf = LAST ? (B + 1) % C::S : B, nks = LAST ? 0 : KSI + 1;
    if constexpr (SYNC) {
        constexpr int inflight = TAIL ? (C::S - B - 2) : (C::S - 2);
        wait_vm_lgkm0<inflight * C::L>();
        wg_barrier();
    }
    static_for<0, C::NM>([&](auto ic) {
        constexpr int i = decltype(ic)::value, ta = i / C::TM, tb = i % C::TM;
        if constexpr (C::SH == 32) acc[ta][tb] = MFMA32(cat8(f.alo[cur][ta], f.ahi[cur][ta]), f.b[cur][tb], acc[ta][tb]);
        else acc[ta][tb] = MFMA16(cat8(f.alo[cur][ta], f.ahi[cur][ta]), f.b[cur][tb], acc[ta][tb]);
        if constexpr (HAS_NEXT) {
            static_for<0, C::NR>([&](auto kc) {
                constexpr int k = decltype(kc)::value;
                if constexpr (read_slot<C>(k) == i) gemm_read_op<C>(st, k, nbuf, nks, nxt, f);
            });
        }
        if constexpr (ISSUE) {
            static_for<0, C::L>([&](auto gc) {
                constexpr int g = decltype(gc)::value;
                if constexpr (glds_slot<C>(g) == i) gemm_glds_op<C>(st, g, t + C::S, B);
            });
        }
        __builtin_amdgcn_sched_barrier(0);
    });
}

template <class C, int B, bool TAIL, int KSI = 0>
DI void gemm_stage_body(const GemmState<C>& st, int t, typename AccT<C::SH>::t (&acc)[C::TN][C::TM], GemmFrags<C>& f) {
    gemm_step<C, B, KSI, TAIL>(st, t, acc, f);
    if constexpr (KSI + 1 < C::KS) gemm_stage_body<C, B, TAIL, KSI + 1>(st, t, acc, f);
}
template <class C, bool TAIL, int B = 0>
DI void gemm_group(const GemmState<C>& st, int t0, typename AccT<C::SH>::t (&acc)[C::TN][C::TM], GemmFrags<C>& f) {
    gemm_stage_body<C, B, TAIL>(st, t0 + B, acc, f);
    if constexpr (B + 1 < C::S) gemm_group<C, TAIL, B + 1>(st, t0, acc, f);
}
template <class C, int B = 0>
DI void gemm_prologue_issue(const GemmState<C>& st) {
#pragma unroll
    for (int g = 0; g < C::L; ++g) gemm_glds_op<C>(st, g, B, B);
    if constexpr (B + 1 < C::S) gemm_prologue_issue<C, B + 1>(st);
}

template <class C>
DI void gemm_core(const char* __restrict__ Xg, int xks, const char* __restrict__ Wg, int ldw, int nt, char* smem,
                  typename AccT<C::SH>::t (&acc)[C::TN][C::TM], unsigned long long& T1) {
    const int tid = threadIdx.x, lane = tid & 63;
    const int wid = __builtin_amdgcn_readfirstlane(tid >> 6);
    const int wm = wid / C::NWN, wn = wid % C::NWN;
    GemmState<C> st;
    st.lds = (char LDSP*)smem;
#pragma unroll
    for (int i = 0; i < C::XPW; ++i) {
        const int pc = wid * C::XPW + i, mt = pc / C::KS16, ks = pc % C::KS16;
        st.xsrc[i] = Xg + ((long)mt * xks + ks) * 1024 + lane * 16;
    }
    {
        const int q4 = (lane >> 2) & 3;
#pragma unroll
        for (int j = 0; j < C::WPW; ++j) {
            const int pc = wid * C::WPW + j, kq = pc / (C::BN / 128), a = pc % (C::BN / 128);
            const int cc = C::SH == 32 ? (lane & 3) : ((lane & 3) ^ (2 * ((kq >> 1) & 1)));
            st.wsrc[j] = Wg + ((long)(4 * kq + q4) * ldw + 128 * a + 32 * (lane >> 4) + 8 * cc) * 2;
        }
    }
    st.xstride = (long)C::KS16 * 1024;
    st.wstride = (long)C::BK * ldw * 2;
    st.xdst = wid * C::XPW * 1024;
    st.wdst = wid * C::WPW * 1024;
    if constexpr (C::SH == 32) {
        const int h = lane >> 5, gg = (lane >> 4) & 1, p = lane & 3, q4 = (lane & 15) >> 2;
        const int n4 = 4 * (p & 1) + 2 * gg + (p >> 1);
        st.a_off[0] = (2 * h * C::NTL) * 256 + q4 * 64 + n4 * 8 + wn * C::TN * 256;
        st.a_off[1] = st.a_off[0];
        st.b_off = lane * 16 + wm * C::TM * C::KS16 * 1024;
    } else {
        const int g = lane >> 4, p = lane & 3, q4 = (lane & 15) >> 2;
        const int base = (2 * g * C::NTL) * 256 + q4 * 64 + p * 8 + wn * (C::TN / 2) * 256;
        st.a_off[0] = base + (g & 1) * 32;
        st.a_off[1] = base + ((g & 1) ^ 1) * 32;
        st.b_off = (g >> 1) * 1024 + (g & 1) * 512 + (lane & 15) * 16 + wm * (C::TM / 2) * C::KS16 * 1024;
    }
    GemmFrags<C> f;
    gemm_prologue_issue<C>(st);
    wait_vm_lgkm0<(C::S - 1) * C::L>();
    wg_barrier();
    T1 = __builtin_amdgcn_s_memtime();
#pragma unroll
    for (int k = 0; k < C::NR; ++k) gemm_read_op<C>(st, k, 0, 0, 0, f);
    __builtin_amdgcn_sched_barrier(0);
    int t0 = 0;
    for (; t0 < nt - C::S; t0 += C::S) gemm_group<C, false>(st, t0, acc, f);
    gemm_group<C, true>(st, t0, acc, f);
}

template <bool RELU>
DI void store_tile_frag(char* base, const f32x16& a, int lane, int  ) {
    const int h = lane >> 5;
    char* p = base + h * 1024 + (lane & 31) * 16;
    unsigned w[8];
#pragma unroll
    for (int i = 0; i < 8; ++i) {
        float lo = a[2 * i], hi = a[2 * i + 1];
        if (RELU) { lo = fmaxf(lo, 0.f); hi = fmaxf(hi, 0.f); }
        w[i] = pk_bf16(lo, hi);
    }
    *(u32x4*)p = (u32x4){w[0], w[1], w[2], w[3]};
    *(u32x4*)(p + 512) = (u32x4){w[4], w[5], w[6], w[7]};
}
typedef unsigned u32x2 __attribute__((ext_vector_type(2)));
template <bool RELU>
DI void store_tile_frag(char* base, const f32x4& a, int lane, int mhalf) {
    const int g = lane >> 4;
    char* p = base + (g >> 1) * 512 + (16 * mhalf + (lane & 15)) * 16 + (g & 1) * 8;
    float v0 = a[0], v1 = a[1], v2 = a[2], v3 = a[3];
    if (RELU) { v0 = fmaxf(v0, 0.f); v1 = fmaxf(v1, 0.f); v2 = fmaxf(v2, 0.f); v3 = fmaxf(v3, 0.f); }
    *(u32x2*)p = (u32x2){pk_bf16(v0, v1), pk_bf16(v2, v3)};
}

constexpr int AT_NKV = 8;
constexpr int AT_KB = 16384;
constexpr int AT_LDS = 8 * AT_KB;

struct AttnState {
    const char* ksrc[2];
    const char* vsrc[2];
    char LDSP* lds;
    unsigned dst;
    unsigned k_off;
    unsigned v_off;
};
DI void attn_issue_k(const AttnState& st, int tile) {
#pragma unroll
    for (int i = 0; i < 2; ++i)
        __builtin_amdgcn_global_load_lds((const unsigned*)(st.ksrc[i] + (long)tile * AT_KB),
                                         (unsigned LDSP*)(st.lds + (tile & 3) * AT_KB + st.dst + i * 1024), 16, 0, 0);
}
DI void attn_issue_v(const AttnState& st, int tile) {
#pragma unroll
    for (int i = 0; i < 2; ++i)
        __builtin_amdgcn_global_load_lds((const unsigned*)(st.vsrc[i] + (long)tile * AT_KB),
                                         (unsigned LDSP*)(st.lds + (4 + (tile & 3)) * AT_KB + st.dst + i * 1024), 16, 0, 0);
}
template <int N> DI void wait_vm() { asm volatile("s_waitcnt vmcnt(%0)" ::"n"(N) : "memory"); }

template <int J, int I>
DI bf16x8 attn_kfrag(const AttnState& st) {
    constexpr int s = I >> 1, kt = I & 1;
    return *(const bf16x8 LDSP*)(st.lds + ((st.k_off ^ (unsigned)(32 * s)) + (unsigned)((J & 3) * AT_KB + kt * 8192)));
}
template <int J, int I, int HALF>
DI s16x4 attn_vfrag(const AttnState& st) {
    constexpr int t = I >> 2, ks = I & 3;
    return tr_read(st.lds + ((J & 3) * AT_KB + ((4 * ks + 2 * HALF) * 4 + t) * 256) + st.v_off);
}
constexpr float AT_C = 0.088388347648318440f * 1.4426950408889634f;

__global__ __launch_bounds__(512, 2) void attn_kernel(const u16* __restrict__ q, const u16* __restrict__ kc, const u16* __restrict__ vc,
                                                      u16* __restrict__ o_out) {
    __shared__ __attribute__((aligned(1024))) char smem[AT_LDS];
    const int tid = threadIdx.x, lane = tid & 63;
    const int wid = __builtin_amdgcn_readfirstlane(tid >> 6);
    const int bid = blockIdx.x;
    const int head = (bid & 7) * 4 + ((bid >> 3) & 3), qb = bid >> 5;
    const int kvh = head >> 3;
    const char* Kg = (const char*)(kc + ((size_t)kvh * 2048 + 1536) * 128);
    const char* Vg = (const char*)(vc + ((size_t)kvh * 2048 + 1536) * 128);
    AttnState st;
    st.lds = (char LDSP*)smem;
    st.dst = wid * 2048;
#pragma unroll
    for (int i = 0; i < 2; ++i) {
        const int pc = wid * 2 + i;
        {
            const int key = 4 * pc + (lane >> 4), c = (lane & 15) ^ (key & 15);
            st.ksrc[i] = Kg + key * 256 + c * 16;
        }
        {
            const int key = 4 * pc + ((lane >> 2) & 3);
            st.vsrc[i] = Vg + key * 256 + ((lane >> 4) * 32 + (lane & 3) * 8) * 2;
        }
    }
    {
        const int h = lane >> 5;
        st.k_off = (lane & 31) * 256 + (((h ^ (lane & 15)) & 15) << 4);
        const int gg = (lane >> 4) & 1, p = lane & 3, q4 = (lane & 15) >> 2;
        const int n4 = 4 * (p & 1) + 2 * gg + (p >> 1);
        st.v_off = 4 * AT_KB + h * 4 * 256 + q4 * 64 + n4 * 8;
        asm volatile("" : "+v"(st.v_off));
    }
    bf16x8 qf[8];
    {
        const char* qp = (const char*)q + ((size_t)(qb * 8 + wid) * 256 + head * 8) * 1024 + lane * 16;
#pragma unroll
        for (int s = 0; s < 8; ++s) qf[s] = *(const bf16x8*)(qp + s * 1024);
    }
    asm volatile("" ::: "memory");
    attn_issue_k(st, 0);
    attn_issue_k(st, 1); attn_issue_v(st, 0);
    attn_issue_k(st, 2); attn_issue_v(st, 1);
    f32x16 o[4];
#pragma unroll
    for (int t = 0; t < 4; ++t) o[t] = (f32x16){};
    float m_run = -INFINITY, l_run = 0.f, alpha = 1.f;
    bf16x8 pf[2][4];
    f32x16 s0, s1;
    bf16x8 kfr[4];
    s16x4 vlo[4], vhi[4];
    float tm[4], mc = 0.f, ps = 0.f;
    constexpr int PFK = 3, PFV = 2;
    auto sm_chunk = [&](auto cc, auto parc) {
        constexpr int c = decltype(cc)::value, PAR = decltype(parc)::value;
        if constexpr (c < 4) {
            tm[c] = fmaxf(fmaxf(fmaxf(s0[4 * c], s0[4 * c + 1]), fmaxf(s0[4 * c + 2], s0[4 * c + 3])),
                          fmaxf(fmaxf(s1[4 * c], s1[4 * c + 1]), fmaxf(s1[4 * c + 2], s1[4 * c + 3])));
        } else if constexpr (c == 4) {
            float tmax = fmaxf(fmaxf(tm[0], tm[1]), fmaxf(tm[2], tm[3]));
            auto rr = __builtin_amdgcn_permlane32_swap(__float_as_uint(tmax), __float_as_uint(tmax), false, false);
            tmax = fmaxf(__uint_as_float(rr[0]), __uint_as_float(rr[1]));
            const float m_new = fmaxf(m_run, tmax);
            alpha = __builtin_amdgcn_exp2f((m_run - m_new) * AT_C);
            m_run = m_new;
            mc = -m_new * AT_C;
            ps = 0.f;
        } else if constexpr (c < 13) {
            constexpr int e = 4 * (c - 5);
#pragma unroll
            for (int u = 0; u < 4; ++u) {
                if (e < 16) { s0[e + u] = __builtin_amdgcn_exp2f(fmaf(s0[e + u], AT_C, mc)); ps += s0[e + u]; }
                else { s1[e - 16 + u] = __builtin_amdgcn_exp2f(fmaf(s1[e - 16 + u], AT_C, mc)); ps += s1[e - 16 + u]; }
            }
        } else {
            if constexpr (c == 13) l_run = l_run * alpha + ps;
            constexpr int lo = c == 13 ? 0 : (c == 14 ? 1 : 3), hi = c == 13 ? 1 : (c == 14 ? 3 : 4);
#pragma unroll
            for (int f = lo; f < hi; ++f) {
                u32x4 w;
#pragma unroll
                for (int i = 0; i < 4; ++i)
                    w[i] = (f >> 1) ? pk_bf16(s1[8 * (f & 1) + 2 * i], s1[8 * (f & 1) + 2 * i + 1]) : pk_bf16(s0[8 * (f & 1) + 2 * i], s0[8 * (f & 1) + 2 * i + 1]);
                pf[PAR][f] = __builtin_bit_cast(bf16x8, w);
            }
        }
    };
    static_for<0, AT_NKV + 1>([&](auto jc) {
        constexpr int j = decltype(jc)::value;
        if constexpr (j < AT_NKV) {
            constexpr int later = (j + 1 < AT_NKV ? 2 : 0) + (j + 0 < AT_NKV ? 2 : 0)
                                + (j + 2 < AT_NKV ? 2 : 0) + (j + 1 < AT_NKV ? 2 : 0);
            wait_vm<later>();
            wg_barrier();
            if constexpr (j + 3 < AT_NKV) attn_issue_k(st, j + 3);
            if constexpr (j + 2 < AT_NKV) attn_issue_v(st, j + 2);
            static_for<0, PFK>([&](auto ic) { constexpr int i = decltype(ic)::value; kfr[i & 3] = attn_kfrag<j, i>(st); });
            __builtin_amdgcn_sched_barrier(0);
            static_for<0, 16>([&](auto ic) {
                constexpr int i = decltype(ic)::value;
                if constexpr (i == 0) s0 = MFMA32(kfr[0], qf[0], (f32x16){});
                else if constexpr (i == 1) s1 = MFMA32(kfr[1], qf[0], (f32x16){});
                else if constexpr (i & 1) s1 = MFMA32(kfr[i & 3], qf[i >> 1], s1);
                else s0 = MFMA32(kfr[i & 3], qf[i >> 1], s0);
                if constexpr (i + PFK < 16) kfr[(i + PFK) & 3] = attn_kfrag<j, (i + PFK < 16 ? i + PFK : 0)>(st);
                else if constexpr (j > 0 && i + PFK - 16 < PFV) {
                    constexpr int iv = i + PFK - 16;
                    vlo[iv & 3] = attn_vfrag<(j > 0 ? j - 1 : 0), iv, 0>(st); vhi[iv & 3] = attn_vfrag<(j > 0 ? j - 1 : 0), iv, 1>(st);
                }
                if constexpr (j >= 2) {
#pragma unroll
                    for (int u = 0; u < 4; ++u) o[i >> 2][4 * (i & 3) + u] *= alpha;
                }
                __builtin_amdgcn_sched_barrier(0);
            });
        } else {
            static_for<0, PFV>([&](auto ic) { constexpr int i = decltype(ic)::value;
                vlo[i & 3] = attn_vfrag<AT_NKV - 1, i, 0>(st); vhi[i & 3] = attn_vfrag<AT_NKV - 1, i, 1>(st); });
#pragma unroll
            for (int t = 0; t < 4; ++t)
#pragma unroll
                for (int r = 0; r < 16; ++r) o[t][r] *= alpha;
            __builtin_amdgcn_sched_barrier(0);
        }
        if constexpr (j > 0) {
            constexpr float dummy = 0.f; (void)dummy;
            static_for<0, 16>([&](auto ic) {
                constexpr int i = decltype(ic)::value;
                o[i >> 2] = MFMA32(cat8(vlo[i & 3], vhi[i & 3]), pf[(j - 1) & 1][i & 3], o[i >> 2]);
                if constexpr (i + PFV < 16) {
                    vlo[(i + PFV) & 3] = attn_vfrag<j - 1, (i + PFV < 16 ? i + PFV : 0), 0>(st);
                    vhi[(i + PFV) & 3] = attn_vfrag<j - 1, (i + PFV < 16 ? i + PFV : 0), 1>(st);
                }
                if constexpr (j < AT_NKV) sm_chunk(ic, std::integral_constant<int, j & 1>{});
                __builtin_amdgcn_sched_barrier(0);
            });
        } else {
            static_for<0, 16>([&](auto ic) { sm_chunk(ic, std::integral_constant<int, 0>{}); });
        }
    });
    float l_tot;
    { auto rr = __builtin_amdgcn_permlane32_swap(__float_as_uint(l_run), __float_as_uint(l_run), false, false);
      l_tot = __uint_as_float(rr[0]) + __uint_as_float(rr[1]); }
    const float inv = 1.0f / l_tot;
    char* base = (char*)o_out + ((size_t)(qb * 8 + wid) * 256 + head * 8) * 1024;
#pragma unroll
    for (int t = 0; t < 4; ++t) {
#pragma unroll
        for (int r = 0; r < 16; ++r) o[t][r] *= inv;
        store_tile_frag<false>(base + t * 2048, o[t], lane, 0);
    }
}

constexpr int M_ = 2048, D_ = 1024, NQ_ = 4096, NF_ = 3584;

__global__ __launch_bounds__(256) void ln_kernel(const u16* __restrict__ x, u16* __restrict__ xn) {
    const int lane = threadIdx.x & 63, wid = threadIdx.x >> 6;
#pragma unroll 1
    for (int rr = 0; rr < 2; ++rr) {
        const int row = blockIdx.x * 8 + wid * 2 + rr;
        const u32x4* src = (const u32x4*)(x + (size_t)row * D_ + lane * 16);
        u32x4 a = src[0], b = src[1];
        float v[16];
#pragma unroll
        for (int i = 0; i < 4; ++i) { v[2 * i] = __uint_as_float(a[i] << 16); v[2 * i + 1] = __uint_as_float(a[i] & 0xffff0000u);
                                      v[8 + 2 * i] = __uint_as_float(b[i] << 16); v[8 + 2 * i + 1] = __uint_as_float(b[i] & 0xffff0000u); }
        float s = 0.f;
#pragma unroll
        for (int i = 0; i < 16; ++i) s += v[i];
#pragma unroll
        for (int o = 32; o >= 1; o >>= 1) s += __shfl_xor(s, o);
        const float mean = s * (1.0f / 1024.0f);
        float q = 0.f;
#pragma unroll
        for (int i = 0; i < 16; ++i) { const float d = v[i] - mean; q += d * d; }
#pragma unroll
        for (int o = 32; o >= 1; o >>= 1) q += __shfl_xor(q, o);
        const float var = q * (1.0f / 1024.0f);
        const float mb = rbf(mean), vb = rbf(var);
        const float den = rbf(sqrtf(rbf(vb + 1e-5f)));
        unsigned o[8];
#pragma unroll
        for (int i = 0; i < 8; ++i) {
            const float d0 = rbf(v[2 * i] - mb), d1 = rbf(v[2 * i + 1] - mb);
            o[i] = pk_bf16(d0 / den, d1 / den);
        }
        char* dst = (char*)xn + ((size_t)(row >> 5) * (D_ / 16) + lane) * 1024 + (row & 31) * 16;
        *(u32x4*)dst = (u32x4){o[0], o[1], o[2], o[3]};
        *(u32x4*)(dst + 512) = (u32x4){o[4], o[5], o[6], o[7]};
    }
}

template <class C, int MINW>
__global__ __launch_bounds__(C::NT, MINW) void gemm1_kernel(const u16* __restrict__ xn, const u16* __restrict__ wq, const u16* __restrict__ w1,
                                                            u16* __restrict__ q, u16* __restrict__ hh) {
    __shared__ __attribute__((aligned(1024))) char smem[C::LDS_BYTES];
    constexpr int NPN = (NQ_ + NF_) / C::BN, NPM = M_ / C::BM, NTILES = NPN * NPM;
    const int bid = blockIdx.x;
    const int per = (NTILES + 7) / 8;
    const int Lid = (bid & 7) * per + (bid >> 3);
    if (Lid >= NTILES || (bid >> 3) >= per) return;
    const int pn = Lid / NPM, pm = Lid % NPM;
    const int n0g = pn * C::BN;
    const bool isq = n0g < NQ_;
    const u16* W = isq ? wq + n0g : w1 + (n0g - NQ_);
    const int ldw = isq ? NQ_ : NF_;
    typename AccT<C::SH>::t acc[C::TN][C::TM];
#pragma unroll
    for (int a = 0; a < C::TN; ++a)
#pragma unroll
        for (int b = 0; b < C::TM; ++b) acc[a][b] = (typename AccT<C::SH>::t){};
    const char* Xg = (const char*)xn + (size_t)(pm * C::BM / 32) * (D_ / 16) * 1024;
    unsigned long long T1;
    gemm_core<C>(Xg, D_ / 16, (const char*)W, ldw, D_ / C::BK, smem, acc, T1);
    int tz = threadIdx.x; asm volatile("" : "+v"(tz));
    const int lane = tz & 63, wid = tz >> 6;
    const int wm = wid / C::NWN, wn = wid % C::NWN;
    char* Y = (char*)(isq ? q : hh);
    const int NY = isq ? NQ_ : NF_;
    const int n0 = isq ? n0g : n0g - NQ_;
#pragma unroll
    for (int ta = 0; ta < C::TN; ++ta)
#pragma unroll
        for (int tb = 0; tb < C::TM; ++tb) {
            const int mrow = pm * C::BM + (wm * C::TM + tb) * C::TT, mt = mrow >> 5, mhalf = (mrow >> 4) & 1;
            const int nb = n0 + (wn * C::TN + ta) * C::TT;
            char* base = Y + ((size_t)mt * (NY / 16) + nb / 16) * 1024;
            if (isq) store_tile_frag<false>(base, acc[ta][tb], lane, mhalf);
            else store_tile_frag<true>(base, acc[ta][tb], lane, mhalf);
        }
}

DI float silu_f(float x) { return x / (1.0f + expf(-x)); }
template <bool SILU> DI float g2_post(float a) { float v = rbf(a); if (SILU) v = silu_f(v); return v; }
template <bool SILU>
DI void store_tile_rm(u16* y  , int ld, const f32x16& a, int lane) {
    u16* p = y + (size_t)(lane & 31) * ld + (lane >> 5) * 16;
    unsigned w[8];
#pragma unroll
    for (int i = 0; i < 8; ++i) w[i] = pk_bf16(g2_post<SILU>(a[2 * i]), g2_post<SILU>(a[2 * i + 1]));
    *(u32x4*)p = (u32x4){w[0], w[1], w[2], w[3]};
    *(u32x4*)(p + 8) = (u32x4){w[4], w[5], w[6], w[7]};
}
template <bool SILU>
DI void store_tile_rm(u16* y, int ld, const f32x4& a, int lane) {
    u16* p = y + (size_t)(lane & 15) * ld + (lane >> 4) * 4;
    *(u32x2*)p = (u32x2){pk_bf16(g2_post<SILU>(a[0]), g2_post<SILU>(a[1])), pk_bf16(g2_post<SILU>(a[2]), g2_post<SILU>(a[3]))};
}
template <class C, int MINW>
__global__ __launch_bounds__(C::NT, MINW) void gemm2_kernel(const u16* __restrict__ o_f, const u16* __restrict__ wo, const u16* __restrict__ h_f,
                                                            const u16* __restrict__ w2, u16* __restrict__ sb, u16* __restrict__ gb) {
    __shared__ __attribute__((aligned(1024))) char smem[C::LDS_BYTES];
    const int bid = blockIdx.x, xcd = bid & 7, i = bid >> 3;
    const int type = i >> 4, r = i & 15;
    const int pm = (xcd >> 1) * 4 + (r & 3), pn = (xcd & 1) * 4 + (r >> 2);
    typename AccT<C::SH>::t acc[C::TN][C::TM];
#pragma unroll
    for (int a = 0; a < C::TN; ++a)
#pragma unroll
        for (int b = 0; b < C::TM; ++b) acc[a][b] = (typename AccT<C::SH>::t){};
    const int Kd = type == 0 ? NQ_ : NF_;
    const char* Xg = (const char*)(type == 0 ? o_f : h_f) + (size_t)(pm * C::BM / 32) * (Kd / 16) * 1024;
    const u16* W = (type == 0 ? wo : w2) + pn * C::BN;
    unsigned long long T1;
    gemm_core<C>(Xg, Kd / 16, (const char*)W, D_, Kd / C::BK, smem, acc, T1);
    int tz = threadIdx.x; asm volatile("" : "+v"(tz));
    const int lane = tz & 63, wid = tz >> 6;
    const int wm = wid / C::NWN, wn = wid % C::NWN;
#pragma unroll
    for (int ta = 0; ta < C::TN; ++ta)
#pragma unroll
        for (int tb = 0; tb < C::TM; ++tb) {
            const int mrow = pm * C::BM + (wm * C::TM + tb) * C::TT;
            const int nb = pn * C::BN + (wn * C::TN + ta) * C::TT;
            if (type == 0) store_tile_rm<false>(sb + (size_t)mrow * D_ + nb, D_, acc[ta][tb], lane);
            else store_tile_rm<true>(gb + (size_t)mrow * D_ + nb, D_, acc[ta][tb], lane);
        }
}
__global__ __launch_bounds__(256) void combine_kernel(const u16* __restrict__ x, const u16* __restrict__ sb, const u16* __restrict__ gb, u16* __restrict__ out) {
    const size_t idx = ((size_t)blockIdx.x * 256 + threadIdx.x);
    const u32x4 xv = ((const u32x4*)x)[idx], sv = ((const u32x4*)sb)[idx], gv = ((const u32x4*)gb)[idx];
    u32x4 ov;
#pragma unroll
    for (int i = 0; i < 4; ++i) {
        const float x0 = __uint_as_float(xv[i] << 16), x1 = __uint_as_float(xv[i] & 0xffff0000u);
        const float s0 = __uint_as_float(sv[i] << 16), s1 = __uint_as_float(sv[i] & 0xffff0000u);
        const float g0 = __uint_as_float(gv[i] << 16), g1 = __uint_as_float(gv[i] & 0xffff0000u);
        ov[i] = pk_bf16(x0 + rbf(s0 * g0), x1 + rbf(s1 * g1));
    }
    ((u32x4*)out)[idx] = ov;
}

#define G1CFG GC<256, 256, 32, 4, 2, 4, 32>
#define G2CFG GC<128, 128, 64, 2, 2, 4, 32>
#define G2MINW 1
using G1 = G1CFG;
using G2 = G2CFG;

constexpr size_t WS_XN = 0, WS_Q = 4u << 20, WS_H = 20u << 20, WS_O = 36u << 20, WS_S = 52u << 20, WS_G = 56u << 20;

extern "C" void kernel_launch(void* const* d_in, const int* in_sizes, int n_in, void* d_out, int out_size, void* d_ws, size_t ws_size,
                              hipStream_t stream) {
    const u16* x = (const u16*)d_in[0];
    const u16* kc = (const u16*)d_in[1];
    const u16* vc = (const u16*)d_in[2];
    const u16* wq = (const u16*)d_in[3];
    const u16* wo = (const u16*)d_in[4];
    const u16* w1 = (const u16*)d_in[6];
    const u16* w2 = (const u16*)d_in[7];
    char* ws = (char*)d_ws;
    u16* xn = (u16*)(ws + WS_XN); u16* q = (u16*)(ws + WS_Q); u16* h = (u16*)(ws + WS_H); u16* o = (u16*)(ws + WS_O);
    u16* sb = (u16*)(ws + WS_S); u16* gb = (u16*)(ws + WS_G);
    ln_kernel<<<256, 256, 0, stream>>>(x, xn);
    gemm1_kernel<G1, 2><<<240, G1::NT, 0, stream>>>(xn, wq, w1, q, h);
    attn_kernel<<<256, 512, 0, stream>>>(q, kc, vc, o);
    gemm2_kernel<G2, G2MINW><<<256, G2::NT, 0, stream>>>(o, wo, h, w2, sb, gb);
    combine_kernel<<<1024, 256, 0, stream>>>(x, sb, gb, (u16*)d_out);
}
```

```cpp
#include <hip/hip_runtime.h>
#include <stdint.h>

typedef short bf16x8 __attribute__((ext_vector_type(8)));
typedef short s16x4 __attribute__((ext_vector_type(4)));
typedef float f32x16 __attribute__((ext_vector_type(16)));
typedef float f32x2 __attribute__((ext_vector_type(2)));
typedef unsigned u32x4 __attribute__((ext_vector_type(4)));
typedef unsigned short u16;
typedef __bf16 bf16x2_t __attribute__((ext_vector_type(2)));
#define LDSP __attribute__((address_space(3)))
#define DI __device__ __forceinline__

DI float bf2f(u16 v) { return __uint_as_float(((unsigned)v) << 16); }
DI unsigned pk_bf16(float lo, float hi) {
    f32x2 f = {lo, hi};
    bf16x2_t b = __builtin_convertvector(f, bf16x2_t);
    return __builtin_bit_cast(unsigned, b);
}
DI float rbf(float x) {
    unsigned p = pk_bf16(x, x);
    return __uint_as_float(p << 16);
}
DI s16x4 tr_read(const char LDSP* p) { return __builtin_amdgcn_ds_read_tr16_b64_v4i16((s16x4 LDSP*)p); }
DI bf16x8 cat8(s16x4 lo, s16x4 hi) { return __builtin_shufflevector(lo, hi, 0, 1, 2, 3, 4, 5, 6, 7); }
#define MFMA32(a, b, c) __builtin_amdgcn_mfma_f32_32x32x16_bf16((a), (b), (c), 0, 0, 0)

#include <type_traits>
template <int I, int N, class F>
DI void static_for(F&& f) {
    if constexpr (I < N) {
        f(std::integral_constant<int, I>{});
        static_for<I + 1, N>(f);
    }
}
typedef float f32x4 __attribute__((ext_vector_type(4)));
#define MFMA16(a, b, c) __builtin_amdgcn_mfma_f32_16x16x32_bf16((a), (b), (c), 0, 0, 0)

template <int BM_, int BN_, int BK_, int NWM_, int NWN_, int S_, int SH_, int ABL_ = 0>
struct GC {
    static constexpr int ABL = ABL_;
    static constexpr int BM = BM_, BN = BN_, BK = BK_, NWM = NWM_, NWN = NWN_, S = S_, SH = SH_;
    static constexpr int NW = NWM * NWN, NT = NW * 64;
    static constexpr int TT = SH, KK = SH == 32 ? 16 : 32;
    static constexpr int TM = BM / TT / NWM, TN = BN / TT / NWN, KS = BK / KK, NTL = BN / 32, KS16 = BK / 16;
    static constexpr int XB = BM * BK * 2, WB = BN * BK * 2, SB = XB + WB;
    static constexpr int XP = XB / 1024, WP = WB / 1024, XPW = XP / NW, WPW = WP / NW, L = XPW + WPW;
    static constexpr int LDS_BYTES = S * SB;
    static constexpr int NM = TN * TM, NR = 2 * TN + TM;
    static_assert(XP % NW == 0 && WP % NW == 0, "pieces per wave");
    static_assert((S * KS) % 2 == 0, "fragment double buffer parity");
    static_assert(BN % 128 == 0, "BN");
    static_assert(SH == 32 || (TM % 2 == 0 && TN % 2 == 0), "TM/TN even for the 16x16 shape");
};
template <int SH> struct AccT;
template <> struct AccT<32> { typedef f32x16 t; };
template <> struct AccT<16> { typedef f32x4 t; };

template <class C>
struct GemmState {
    const char* xsrc[C::XPW];
    const char* wsrc[C::WPW];
    long xstride, wstride;
    char LDSP* lds;
    unsigned xdst, wdst;
    unsigned a_off[2], b_off;
};

template <int N> DI void wait_vm_lgkm0() { asm volatile("s_waitcnt vmcnt(%0) lgkmcnt(0)" ::"n"(N) : "memory"); }
DI void wg_barrier() {
    asm volatile("" ::: "memory");
    __builtin_amdgcn_s_barrier();
    asm volatile("" ::: "memory");
}

template <class C>
struct GemmFrags {
    s16x4 alo[2][C::TN], ahi[2][C::TN];
    bf16x8 b[2][C::TM];
};
template <class C>
DI void gemm_read_op(const GemmState<C>& st, int k, int buf, int ks, int sel, GemmFrags<C>& f) {
    if (k >= 2 && k < 2 + C::TM) {
        const int tb = k - 2;
        if constexpr (C::SH == 32)
            f.b[sel][tb] = *(const bf16x8 LDSP*)(st.lds + (buf * C::SB + (tb * C::KS16 + ks) * 1024) + st.b_off);
        else
            f.b[sel][tb] = *(const bf16x8 LDSP*)(st.lds + (buf * C::SB + ((tb >> 1) * C::KS16 + 2 * ks) * 1024 + (tb & 1) * 256) + st.b_off);
    } else {
        const int idx = k < 2 ? k : k - C::TM, ta = idx >> 1, half = idx & 1;
        const char LDSP* p;
        if constexpr (C::SH == 32) p = st.lds + (buf * C::SB + C::XB + ((4 * ks + half) * C::NTL + ta) * 256) + st.a_off[0];
        else p = st.lds + (buf * C::SB + C::XB + ((8 * ks + half) * C::NTL + (ta >> 1)) * 256) + st.a_off[ta & 1];
        if (half) f.ahi[sel][ta] = tr_read(p); else f.alo[sel][ta] = tr_read(p);
    }
}
template <class C>
DI void gemm_glds_op(const GemmState<C>& st, int g, int t, int buf) {
    if (g < C::XPW)
        __builtin_amdgcn_global_load_lds((const unsigned*)(st.xsrc[g] + (long)t * st.xstride),
                                         (unsigned LDSP*)(st.lds + buf * C::SB + st.xdst + g * 1024), 16, 0, 0);
    else
        __builtin_amdgcn_global_load_lds((const unsigned*)(st.wsrc[g - C::XPW] + (long)t * st.wstride),
                                         (unsigned LDSP*)(st.lds + buf * C::SB + C::XB + st.wdst + (g - C::XPW) * 1024), 16, 0, 0);
}
template <class C> constexpr int read_slot(int k) {
    constexpr int SPAN = C::NM - (C::NM / 4 > 1 ? C::NM / 4 : 1);
    return k * SPAN / C::NR;
}
template <class C> constexpr int glds_slot(int g) {
    constexpr int OFF = C::NM / 4;
    return OFF + g * (C::NM - OFF) / C::L;
}

template <class C, int B, int KSI, bool TAIL>
DI void gemm_step(const GemmState<C>& st, int t, typename AccT<C::SH>::t (&acc)[C::TN][C::TM], GemmFrags<C>& f) {
    constexpr int cur = (C::ABL & 1) ? 0 : ((B * C::KS + KSI) & 1), nxt = cur ^ 1;
    constexpr bool LAST = KSI == C::KS - 1;
    constexpr bool HAS_NEXT = !(LAST && TAIL && B == C::S - 1);
    constexpr bool SYNC = LAST && HAS_NEXT, ISSUE = SYNC && !TAIL;
    constexpr int nbuf = LAST ? (B + 1) % C::S : B, nks = LAST ? 0 : KSI + 1;
    if constexpr (SYNC) {
        constexpr int inflight = TAIL ? (C::S - B - 2) : (C::S - 2);
        wait_vm_lgkm0<inflight * C::L>();
        wg_barrier();
    }
    static_for<0, C::NM>([&](auto ic) {
        constexpr int i = decltype(ic)::value, ta = i / C::TM, tb = i % C::TM;
        if constexpr (C::ABL & 4) { asm volatile("" :: "v"(f.alo[cur][ta]), "v"(f.ahi[cur][ta]), "v"(f.b[cur][tb])); }
        else if constexpr (C::SH == 32) acc[ta][tb] = MFMA32(cat8(f.alo[cur][ta], f.ahi[cur][ta]), f.b[cur][tb], acc[ta][tb]);
        else acc[ta][tb] = MFMA16(cat8(f.alo[cur][ta], f.ahi[cur][ta]), f.b[cur][tb], acc[ta][tb]);
        if constexpr (HAS_NEXT && !(C::ABL & 1)) {
            static_for<0, C::NR>([&](auto kc) {
                constexpr int k = decltype(kc)::value;
                if constexpr (read_slot<C>(k) == i) gemm_read_op<C>(st, k, nbuf, nks, nxt, f);
            });
        }
        if constexpr (ISSUE && !(C::ABL & 2)) {
            static_for<0, C::L>([&](auto gc) {
                constexpr int g = decltype(gc)::value;
                if constexpr (glds_slot<C>(g) == i) gemm_glds_op<C>(st, g, t + C::S, B);
            });
        }
        __builtin_amdgcn_sched_barrier(0);
    });
}

template <class C, int B, bool TAIL, int KSI = 0>
DI void gemm_stage_body(const GemmState<C>& st, int t, typename AccT<C::SH>::t (&acc)[C::TN][C::TM], GemmFrags<C>& f) {
    gemm_step<C, B, KSI, TAIL>(st, t, acc, f);
    if constexpr (KSI + 1 < C::KS) gemm_stage_body<C, B, TAIL, KSI + 1>(st, t, acc, f);
}
template <class C, bool TAIL, int B = 0>
DI void gemm_group(const GemmState<C>& st, int t0, typename AccT<C::SH>::t (&acc)[C::TN][C::TM], GemmFrags<C>& f) {
    gemm_stage_body<C, B, TAIL>(st, t0 + B, acc, f);
    if constexpr (B + 1 < C::S) gemm_group<C, TAIL, B + 1>(st, t0, acc, f);
}
template <class C, int B = 0>
DI void gemm_prologue_issue(const GemmState<C>& st) {
#pragma unroll
    for (int g = 0; g < C::L; ++g) gemm_glds_op<C>(st, g, B, B);
    if constexpr (B + 1 < C::S) gemm_prologue_issue<C, B + 1>(st);
}

template <class C>
DI void gemm_core(const char* __restrict__ Xg, int xks, const char* __restrict__ Wg, int ldw, int nt, char* smem,
                  typename AccT<C::SH>::t (&acc)[C::TN][C::TM], unsigned long long& T1) {
    const int tid = threadIdx.x, lane = tid & 63;
    const int wid = __builtin_amdgcn_readfirstlane(tid >> 6);
    const int wm = wid / C::NWN, wn = wid % C::NWN;
    GemmState<C> st;
    st.lds = (char LDSP*)smem;
#pragma unroll
    for (int i = 0; i < C::XPW; ++i) {
        const int pc = wid * C::XPW + i, mt = pc / C::KS16, ks = pc % C::KS16;
        st.xsrc[i] = Xg + ((long)mt * xks + ks) * 1024 + lane * 16;
    }
    {
        const int q4 = (lane >> 2) & 3;
#pragma unroll
        for (int j = 0; j < C::WPW; ++j) {
            const int pc = wid * C::WPW + j, kq = pc / (C::BN / 128), a = pc % (C::BN / 128);
            const int cc = C::SH == 32 ? (lane & 3) : ((lane & 3) ^ (2 * ((kq >> 1) & 1)));
            st.wsrc[j] = Wg + ((long)(4 * kq + q4) * ldw + 128 * a + 32 * (lane >> 4) + 8 * cc) * 2;
        }
    }
    st.xstride = (long)C::KS16 * 1024;
    st.wstride = (long)C::BK * ldw * 2;
    st.xdst = wid * C::XPW * 1024;
    st.wdst = wid * C::WPW * 1024;
    if constexpr (C::SH == 32) {
        const int h = lane >> 5, gg = (lane >> 4) & 1, p = lane & 3, q4 = (lane & 15) >> 2;
        const int n4 = 4 * (p & 1) + 2 * gg + (p >> 1);
        st.a_off[0] = (2 * h * C::NTL) * 256 + q4 * 64 + n4 * 8 + wn * C::TN * 256;
        st.a_off[1] = st.a_off[0];
        st.b_off = lane * 16 + wm * C::TM * C::KS16 * 1024;
    } else {
        const int g = lane >> 4, p = lane & 3, q4 = (lane & 15) >> 2;
        const int base = (2 * g * C::NTL) * 256 + q4 * 64 + p * 8 + wn * (C::TN / 2) * 256;
        st.a_off[0] = base + (g & 1) * 32;
        st.a_off[1] = base + ((g & 1) ^ 1) * 32;
        st.b_off = (g >> 1) * 1024 + (g & 1) * 512 + (lane & 15) * 16 + wm * (C::TM / 2) * C::KS16 * 1024;
    }
    GemmFrags<C> f;
    gemm_prologue_issue<C>(st);
    wait_vm_lgkm0<(C::S - 1) * C::L>();
    wg_barrier();
    T1 = __builtin_amdgcn_s_memtime();
#pragma unroll
    for (int k = 0; k < C::NR; ++k) gemm_read_op<C>(st, k, 0, 0, 0, f);
    __builtin_amdgcn_sched_barrier(0);
    int t0 = 0;
    for (; t0 < nt - C::S; t0 += C::S) gemm_group<C, false>(st, t0, acc, f);
    gemm_group<C, true>(st, t0, acc, f);
}

template <bool RELU>
DI void store_tile_frag(char* base, const f32x16& a, int lane, int  ) {
    const int h = lane >> 5;
    char* p = base + h * 1024 + (lane & 31) * 16;
    unsigned w[8];
#pragma unroll
    for (int i = 0; i < 8; ++i) {
        float lo = a[2 * i], hi = a[2 * i + 1];
        if (RELU) { lo = fmaxf(lo, 0.f); hi = fmaxf(hi, 0.f); }
        w[i] = pk_bf16(lo, hi);
    }
    *(u32x4*)p = (u32x4){w[0], w[1], w[2], w[3]};
    *(u32x4*)(p + 512) = (u32x4){w[4], w[5], w[6], w[7]};
}
typedef unsigned u32x2 __attribute__((ext_vector_type(2)));
template <bool RELU>
DI void store_tile_frag(char* base, const f32x4& a, int lane, int mhalf) {
    const int g = lane >> 4;
    char* p = base + (g >> 1) * 512 + (16 * mhalf + (lane & 15)) * 16 + (g & 1) * 8;
    float v0 = a[0], v1 = a[1], v2 = a[2], v3 = a[3];
    if (RELU) { v0 = fmaxf(v0, 0.f); v1 = fmaxf(v1, 0.f); v2 = fmaxf(v2, 0.f); v3 = fmaxf(v3, 0.f); }
    *(u32x2*)p = (u32x2){pk_bf16(v0, v1), pk_bf16(v2, v3)};
}

template <int BM_, int BN_, int NW_, int S_, int SH_>
struct DC {
    static constexpr int BM = BM_, BN = BN_, NW = NW_, S = S_, SH = SH_, BK = 64;
    static constexpr int NT = NW * 64;
    static constexpr int TT = SH, KK = SH == 32 ? 16 : 32;
    static constexpr int TW = BM / NW, TM = TW / TT, TN = BN / TT, KS = BK / KK, NTL = BN / 32;
    static constexpr int TM32 = TW / 32;
    static constexpr int WB = BN * BK * 2, WP = WB / 1024, LW = WP / NW;
    static constexpr int LB = TM * KS;
    static constexpr int LG = LW + LB;
    static constexpr int LDS_BYTES = S * WB;
    static constexpr int NM = TN * TM;
    static_assert(WP % NW == 0, "W pieces per wave");
    static_assert(BN % 128 == 0 && TW % 32 == 0, "shape");
    static_assert(SH == 32 || TN % 2 == 0, "TN even for the 16x16 shape");
    static_assert((S - 2) * LG <= 63, "vmcnt range");
};

template <class C>
struct DGState {
    const char* wbase;
    unsigned woff[C::LW];
    const char* xbase;
    unsigned xoff[C::TM32];
    long wstride;
    char LDSP* lds;
    unsigned lds_u;
    unsigned wdst;
    unsigned a_off[2];
};
template <class C>
struct DGFrags {
    s16x4 alo[2][C::TN], ahi[2][C::TN];
    bf16x8 b[C::S][C::KS][C::TM];
};

DI void glds16_asm(unsigned lds_addr, const char* sbase, unsigned voff) {
    asm volatile("s_mov_b32 m0, %0\n\ts_nop 0\n\tglobal_load_lds_dwordx4 %1, %2" :: "s"(lds_addr), "v"(voff), "s"(sbase) : "memory");
}
template <class C>
DI void dg_glds(const DGState<C>& st, int g, int t, int slot) {
    glds16_asm(st.lds_u + slot * C::WB + st.wdst + g * 1024, st.wbase + (long)t * st.wstride, st.woff[g]);
}
template <bool AG> DI void mfma_asm(f32x4& acc, const bf16x8& a, const bf16x8& b) {
    if constexpr (AG) asm volatile("v_mfma_f32_16x16x32_bf16 %0, %1, %2, %0" : "+a"(acc) : "v"(a), "v"(b));
    else asm volatile("v_mfma_f32_16x16x32_bf16 %0, %1, %2, %0" : "+v"(acc) : "v"(a), "v"(b));
}
template <bool AG> DI void mfma_asm(f32x16& acc, const bf16x8& a, const bf16x8& b) {
    if constexpr (AG) asm volatile("v_mfma_f32_32x32x16_bf16 %0, %1, %2, %0" : "+a"(acc) : "v"(a), "v"(b));
    else asm volatile("v_mfma_f32_32x32x16_bf16 %0, %1, %2, %0" : "+v"(acc) : "v"(a), "v"(b));
}
template <bool AG, class T, int TN, int TM>
DI void dg_zero_acc(T (&acc)[TN][TM]) {
#pragma unroll
    for (int a = 0; a < TN; ++a)
#pragma unroll
        for (int b = 0; b < TM; ++b) {
            acc[a][b] = (T){};
            if constexpr (AG) asm volatile("" : "+a"(acc[a][b]));
            else asm volatile("" : "+v"(acc[a][b]));
        }
    asm volatile("s_nop 1");
}
template <bool AG, class T, int TN, int TM>
DI void dg_drain_mfma(T (&acc)[TN][TM]) {
    asm volatile("s_nop 15");
#pragma unroll
    for (int a = 0; a < TN; ++a)
#pragma unroll
        for (int b = 0; b < TM; ++b) {
            if constexpr (AG) asm volatile("" : "+a"(acc[a][b]));
            else asm volatile("" : "+v"(acc[a][b]));
        }
    __builtin_amdgcn_sched_barrier(0);
}
template <int IMM>
DI void gload16_asm(bf16x8& dst, const char* sbase, unsigned voff) {
    asm volatile("global_load_dwordx4 %0, %1, %2 offset:%3" : "=v"(dst) : "v"(voff), "s"(sbase), "n"(IMM) : "memory");
}
template <class C, int BUF, int D>
DI void dg_bload(const DGState<C>& st, int t, DGFrags<C>& f) {
    constexpr int ks = D / C::TM, tb = D % C::TM;
    const char* sb = st.xbase + (long)t * 4096;
    if constexpr (C::SH == 32) gload16_asm<ks * 1024>(f.b[BUF][ks][tb], sb, st.xoff[tb]);
    else gload16_asm<(tb & 1) * 256 + ks * 2048>(f.b[BUF][ks][tb], sb, st.xoff[tb >> 1]);
}
template <class C>
DI void dg_aread(const DGState<C>& st, int k, int slot, int ks, int sel, DGFrags<C>& f) {
    const int ta = k >> 1, half = k & 1;
    const char LDSP* p;
    if constexpr (C::SH == 32) p = st.lds + (slot * C::WB + ((4 * ks + half) * C::NTL + ta) * 256) + st.a_off[0];
    else p = st.lds + (slot * C::WB + ((8 * ks + half) * C::NTL + (ta >> 1)) * 256) + st.a_off[ta & 1];
    if (half) f.ahi[sel][ta] = tr_read(p); else f.alo[sel][ta] = tr_read(p);
}
#define DG_DRAIN 0
template <class C, int N0, int BUF>
DI void dg_wait(DGFrags<C>& f) {
    constexpr int N = DG_DRAIN ? 0 : N0;
    if constexpr (C::LB == 4) {
        constexpr int T = C::TM;
        asm volatile("s_waitcnt vmcnt(%4) lgkmcnt(0)"
                     : "+v"(f.b[BUF][0 / T][0 % T]), "+v"(f.b[BUF][1 / T][1 % T]), "+v"(f.b[BUF][2 / T][2 % T]), "+v"(f.b[BUF][3 / T][3 % T])
                     : "n"(N) : "memory");
    } else {
        static_assert(C::LB == 8, "LB");
        constexpr int T = C::TM;
        asm volatile("s_waitcnt vmcnt(%8) lgkmcnt(0)"
                     : "+v"(f.b[BUF][0 / T][0 % T]), "+v"(f.b[BUF][1 / T][1 % T]), "+v"(f.b[BUF][2 / T][2 % T]), "+v"(f.b[BUF][3 / T][3 % T]),
                       "+v"(f.b[BUF][4 / T][4 % T]), "+v"(f.b[BUF][5 / T][5 % T]), "+v"(f.b[BUF][6 / T][6 % T]), "+v"(f.b[BUF][7 / T][7 % T])
                     : "n"(N) : "memory");
    }
}
template <class C> constexpr int dg_aslot(int k) {
    return k * (C::NM - (C::NM / 4 > 1 ? C::NM / 4 : 1)) / (2 * C::TN);
}

template <class C, bool AG, bool TAIL, int B, int KSI, int EXTRA = 0>
DI void dg_step(const DGState<C>& st, int t, typename AccT<C::SH>::t (&acc)[C::TN][C::TM], DGFrags<C>& f) {
    constexpr int cur = (B * C::KS + KSI) & 1, nxt = cur ^ 1;
    constexpr bool LAST = KSI == C::KS - 1, FIRST = KSI == 0;
    constexpr bool HAS_NEXT = !(LAST && TAIL && B == C::S - 1);
    constexpr int nslot = LAST ? (B + 1) % C::S : B, nks = LAST ? 0 : KSI + 1;
    constexpr bool HAS_P = FIRST && (!TAIL || B == 0);
    constexpr int pslot = (B + C::S - 1) % C::S;
    if constexpr (LAST && HAS_NEXT) {
        dg_wait<C, (TAIL ? C::S - B - 2 : C::S - 2) * C::LG + EXTRA, (B + 1) % C::S>(f);
        wg_barrier();
    }
    static_for<0, C::NM>([&](auto ic) {
        constexpr int i = decltype(ic)::value, ta = i / C::TM, tb = i % C::TM;
        mfma_asm<AG>(acc[ta][tb], cat8(f.alo[cur][ta], f.ahi[cur][ta]), f.b[B][KSI][tb]);
        if constexpr (HAS_NEXT) {
            static_for<0, 2 * C::TN>([&](auto kc) {
                constexpr int k = decltype(kc)::value;
                if constexpr (dg_aslot<C>(k) == i) dg_aread<C>(st, k, nslot, nks, nxt, f);
            });
        }
        if constexpr (HAS_P) {
            static_for<0, C::LB>([&](auto dc) {
                constexpr int d = decltype(dc)::value;
                if constexpr (d * C::NM / C::LG == i) dg_bload<C, pslot, d>(st, t + C::S - 1, f);
            });
            static_for<0, C::LW>([&](auto gc) {
                constexpr int g = decltype(gc)::value;
                if constexpr ((C::LB + g) * C::NM / C::LG == i) dg_glds<C>(st, g, t + C::S - 1, pslot);
            });
        }
        __builtin_amdgcn_sched_barrier(0);
    });
}
template <class C, bool AG, bool TAIL, int B, int EXTRA = 0, int KSI = 0>
DI void dg_stage(const DGState<C>& st, int t, typename AccT<C::SH>::t (&acc)[C::TN][C::TM], DGFrags<C>& f) {
    dg_step<C, AG, TAIL, B, KSI, EXTRA>(st, t, acc, f);
    if constexpr (KSI + 1 < C::KS) dg_stage<C, AG, TAIL, B, EXTRA, KSI + 1>(st, t, acc, f);
}
template <class C, bool AG, bool TAIL, int B = 0>
DI void dg_group(const DGState<C>& st, int t0, typename AccT<C::SH>::t (&acc)[C::TN][C::TM], DGFrags<C>& f) {
    dg_stage<C, AG, TAIL, B>(st, t0 + B, acc, f);
    if constexpr (B + 1 < C::S) dg_group<C, AG, TAIL, B + 1>(st, t0, acc, f);
}

template <class C>
DI void dg_setup(DGState<C>& st, const char* Xw, int xks, const char* Wg, int ldw, char LDSP* lds) {
    const int tid = threadIdx.x, lane = tid & 63;
    const int wid = __builtin_amdgcn_readfirstlane(tid >> 6);
    st.lds = lds;
    const int q4 = (lane >> 2) & 3;
#pragma unroll
    for (int j = 0; j < C::LW; ++j) {
        const int pc = wid * C::LW + j, kq = pc / (C::BN / 128), a = pc % (C::BN / 128);
        const int cc = C::SH == 32 ? (lane & 3) : ((lane & 3) ^ (2 * ((kq >> 1) & 1)));
        st.woff[j] = (unsigned)(((4 * kq + q4) * ldw + 128 * a + 32 * (lane >> 4) + 8 * cc) * 2);
    }
    st.wbase = Wg;
    st.lds_u = (unsigned)(size_t)lds;
    st.wstride = (long)C::BK * ldw * 2;
    st.wdst = wid * C::LW * 1024;
    st.xbase = Xw;
    if constexpr (C::SH == 32) {
#pragma unroll
        for (int i = 0; i < C::TM32; ++i) st.xoff[i] = lane * 16 + i * xks * 1024;
        const int h = lane >> 5, gg = (lane >> 4) & 1, p = lane & 3, q4r = (lane & 15) >> 2;
        const int n4 = 4 * (p & 1) + 2 * gg + (p >> 1);
        st.a_off[0] = st.a_off[1] = (2 * h * C::NTL) * 256 + q4r * 64 + n4 * 8;
    } else {
        const int g = lane >> 4, p = lane & 3, q4r = (lane & 15) >> 2;
#pragma unroll
        for (int i = 0; i < C::TM32; ++i) st.xoff[i] = (g >> 1) * 1024 + (g & 1) * 512 + (lane & 15) * 16 + i * xks * 1024;
        const int base = (2 * g * C::NTL) * 256 + q4r * 64 + p * 8;
        st.a_off[0] = base + (g & 1) * 32;
        st.a_off[1] = base + ((g & 1) ^ 1) * 32;
    }
}
template <class C>
DI void dg_prologue(const DGState<C>& st, DGFrags<C>& f) {
    static_for<0, C::S - 1>([&](auto ic) {
        constexpr int i = decltype(ic)::value;
        static_for<0, C::LB>([&](auto dc) { dg_bload<C, i, decltype(dc)::value>(st, i, f); });
#pragma unroll
        for (int g = 0; g < C::LW; ++g) dg_glds<C>(st, g, i, i);
    });
}
template <class C, int EXTRA = 0>
DI void dg_start(const DGState<C>& st, DGFrags<C>& f) {
    dg_wait<C, (C::S - 2) * C::LG + EXTRA, 0>(f);
    wg_barrier();
    static_for<0, 2 * C::TN>([&](auto kc) { dg_aread<C>(st, decltype(kc)::value, 0, 0, 0, f); });
    __builtin_amdgcn_sched_barrier(0);
}
template <class C, bool AG>
DI void dg_run(const DGState<C>& st, int nt, typename AccT<C::SH>::t (&acc)[C::TN][C::TM], DGFrags<C>& f) {
    dg_start<C>(st, f);
    int t0 = 0;
    for (; t0 < nt - C::S; t0 += C::S) dg_group<C, AG, false>(st, t0, acc, f);
    dg_group<C, AG, true>(st, t0, acc, f);
    dg_drain_mfma<AG>(acc);
}

using K2Q = DC<256, 128, 8, 4, 32>;
using K2F = DC<256, 128, 8, 4, 16>;
#define K2_ABL 0
constexpr int K2_LDS = 160 * 1024;
constexpr int K2_TB = 16384;
DI constexpr int k2_kslot(int j) { return 4 + j % 3; }
DI constexpr int k2_vslot(int j) { return ((j + 1) & 3) == 3 ? 3 : 7 + ((j + 1) & 3); }
constexpr float K2_C = 0.088388347648318440f * 1.4426950408889634f;

struct K2Attn {
    const char* kbase; const char* vbase;
    unsigned koff[2], voff[2];
    unsigned lds_u, dst;
    char LDSP* lds;
    unsigned k_addr, v_addrA, v_addrB;
};
DI void k2_issue_k(const K2Attn& a, int j) {
#pragma unroll
    for (int i = 0; i < 2; ++i) glds16_asm(a.lds_u + k2_kslot(j) * K2_TB + a.dst + i * 1024, a.kbase + (long)j * K2_TB, a.koff[i]);
}
DI void k2_issue_v(const K2Attn& a, int j) {
#pragma unroll
    for (int i = 0; i < 2; ++i) glds16_asm(a.lds_u + k2_vslot(j) * K2_TB + a.dst + i * 1024, a.vbase + (long)j * K2_TB, a.voff[i]);
}
template <int J, int I>
DI bf16x8 k2_kfrag(const K2Attn& a) {
    constexpr int u = I >> 1, kt = I & 1, cu = 4 * (u >> 1) + (u & 1);
    return *(const bf16x8 LDSP*)(a.lds + ((a.k_addr ^ (unsigned)(cu << 4)) + (unsigned)((k2_kslot(J) - 4) * K2_TB + kt * 8192)));
}
template <int J, int I, int HALF>
DI s16x4 k2_vfrag(const K2Attn& a) {
    constexpr int t = I & 3, ks = I >> 2, sl = k2_vslot(J);
    constexpr int off = ((4 * ks + 2 * HALF) * 4 + t) * 256;
    if constexpr (sl == 3) return tr_read(a.lds + off + a.v_addrB);
    else return tr_read(a.lds + ((sl - 6) * K2_TB + off) + a.v_addrA);
}
template <int N> DI void wait_vm() { asm volatile("s_waitcnt vmcnt(%0)" ::"n"(N) : "memory"); }

struct K2Soft {
    f32x16 o[4];
    float m_run, l_run, alpha, mc, ps, tm;
    u32x4 pf[2][4];
    f32x16 T[3];
    bf16x8 kfr[4];
    s16x4 vlo[4], vhi[4];
};
DI float max3_asm(float a, float b, float c) { float r; asm volatile("v_max3_f32 %0, %1, %2, %3" : "=v"(r) : "v"(a), "v"(b), "v"(c)); return r; }
template <class T> DI void anchor(T& x) { asm volatile("" : "+v"(x)); }
template <int PAR, int F, int W>
DI void k2_exp4(K2Soft& z, float s0, float s1, float s2, float s3) {
    const float p0 = __builtin_amdgcn_exp2f(fmaf(s0, K2_C, z.mc)), p1 = __builtin_amdgcn_exp2f(fmaf(s1, K2_C, z.mc));
    const float p2 = __builtin_amdgcn_exp2f(fmaf(s2, K2_C, z.mc)), p3 = __builtin_amdgcn_exp2f(fmaf(s3, K2_C, z.mc));
    z.ps += (p0 + p1) + (p2 + p3);
    unsigned k0 = pk_bf16(p0, p1), k1 = pk_bf16(p2, p3);
    anchor(z.ps); anchor(k0); anchor(k1);
    z.pf[PAR][F][W] = k0;
    z.pf[PAR][F][W + 1] = k1;
}
template <int j, int c>
DI void k2_sm_start(K2Soft& z) {
    f32x16& sa = z.T[(2 * j) % 3]; f32x16& sb = z.T[(2 * j + 1) % 3];
    if constexpr (c < 4) {
        float t = c == 0 ? max3_asm(sa[0], sa[1], sa[2]) : max3_asm(z.tm, sa[4 * c], sa[4 * c + 1]);
        t = c == 0 ? max3_asm(t, sa[3], sa[3]) : max3_asm(t, sa[4 * c + 2], sa[4 * c + 3]);
        t = max3_asm(t, sb[4 * c], sb[4 * c + 1]);
        z.tm = max3_asm(t, sb[4 * c + 2], sb[4 * c + 3]);
    } else if constexpr (c == 4) {
        auto rr = __builtin_amdgcn_permlane32_swap(__float_as_uint(z.tm), __float_as_uint(z.tm), false, false);
        const float m_new = fmaxf(z.m_run, fmaxf(__uint_as_float(rr[0]), __uint_as_float(rr[1])));
        z.alpha = __builtin_amdgcn_exp2f((z.m_run - m_new) * K2_C);
        z.m_run = m_new;
        z.mc = -m_new * K2_C;
        z.ps = 0.f;
        anchor(z.alpha); anchor(z.mc);
    } else if constexpr (c == 5 || c == 7 || c == 9 || c == 11) {
        constexpr int k = (c - 5) / 2;
        k2_exp4<j & 1, (k >> 1), 2 * (k & 1)>(z, sa[4 * k], sa[4 * k + 1], sa[4 * k + 2], sa[4 * k + 3]);
    }
}
template <int j, int c>
DI void k2_sm_finish(K2Soft& z) {
    f32x16& sb = z.T[(2 * j + 1) % 3];
    if constexpr (c == 0 || c == 2 || c == 4 || c == 6) {
        constexpr int k = c / 2;
        k2_exp4<j & 1, 2 + (k >> 1), 2 * (k & 1)>(z, sb[4 * k], sb[4 * k + 1], sb[4 * k + 2], sb[4 * k + 3]);
    } else if constexpr (c == 8) {
        z.l_run = z.l_run * z.alpha + z.ps;
        anchor(z.l_run);
    }
}
template <int j>
DI void k2_phaseA(const K2Attn& a, const bf16x8 (&qf)[8], K2Soft& z) {
    constexpr int PFK = 3;
    f32x16& sa = z.T[(2 * j) % 3]; f32x16& sb = z.T[(2 * j + 1) % 3];
    if constexpr (j < 8) {
        static_for<0, PFK>([&](auto ic) { constexpr int i = decltype(ic)::value; z.kfr[i & 3] = k2_kfrag<(j < 8 ? j : 0), i>(a); });
        __builtin_amdgcn_sched_barrier(0);
    }
    static_for<0, 16>([&](auto ic) {
        constexpr int i = decltype(ic)::value;
        if constexpr (j < 8) {
            if constexpr (i == 0) sa = MFMA32(z.kfr[0], qf[0], (f32x16){});
            else if constexpr (i == 1) sb = MFMA32(z.kfr[1], qf[0], (f32x16){});
            else if constexpr (i & 1) sb = MFMA32(z.kfr[i & 3], qf[i >> 1], sb);
            else sa = MFMA32(z.kfr[i & 3], qf[i >> 1], sa);
#if !(K2_ABL & 2)
            if constexpr (i + PFK < 16) z.kfr[(i + PFK) & 3] = k2_kfrag<(j < 8 ? j : 0), (i + PFK < 16 ? i + PFK : 0)>(a);
#endif
        }
#if !(K2_ABL & 1)
        if constexpr (j >= 1) k2_sm_finish<j - 1, i>(z);
#endif
        if constexpr (j >= 2 && !(K2_ABL & 4)) {
#pragma unroll
            for (int u = 0; u < 4; ++u) { float v = z.o[i >> 2][4 * (i & 3) + u] * z.alpha; anchor(v); z.o[i >> 2][4 * (i & 3) + u] = v; }
        }
        __builtin_amdgcn_sched_barrier(0);
    });
}
template <int j>
DI void k2_phaseB(const K2Attn& a, K2Soft& z) {
    constexpr int PFV = 2;
    if constexpr (j >= 1) {
        static_for<0, PFV>([&](auto ic) { constexpr int i = decltype(ic)::value;
            z.vlo[i & 3] = k2_vfrag<(j >= 1 ? j - 1 : 0), i, 0>(a); z.vhi[i & 3] = k2_vfrag<(j >= 1 ? j - 1 : 0), i, 1>(a); });
    }
    if constexpr (j < 8) asm volatile("s_nop 11");
    __builtin_amdgcn_sched_barrier(0);
    static_for<0, 16>([&](auto ic) {
        constexpr int i = decltype(ic)::value;
        if constexpr (j >= 1) {
            z.o[i & 3] = MFMA32(cat8(z.vlo[i & 3], z.vhi[i & 3]), __builtin_bit_cast(bf16x8, z.pf[(j - 1) & 1][i >> 2]), z.o[i & 3]);
#if !(K2_ABL & 2)
            if constexpr (i + PFV < 16) {
                z.vlo[(i + PFV) & 3] = k2_vfrag<(j >= 1 ? j - 1 : 0), (i + PFV < 16 ? i + PFV : 0), 0>(a);
                z.vhi[(i + PFV) & 3] = k2_vfrag<(j >= 1 ? j - 1 : 0), (i + PFV < 16 ? i + PFV : 0), 1>(a);
            }
#endif
        }
#if !(K2_ABL & 1)
        if constexpr (j < 8) k2_sm_start<(j < 8 ? j : 0), i>(z);
#else
        if constexpr (j < 8 && i == 5) {
#pragma unroll
            for (int f = 0; f < 4; ++f)
#pragma unroll
                for (int w = 0; w < 4; ++w) z.pf[j & 1][f][w] = __float_as_uint(f < 2 ? z.T[(2 * j) % 3][4 * f + w] : z.T[(2 * j + 1) % 3][4 * f + w]);
        }
#endif
        __builtin_amdgcn_sched_barrier(0);
    });
}

__global__ __launch_bounds__(512, 2) void k2_kernel(const u16* __restrict__ xn, const u16* __restrict__ wq, const u16* __restrict__ kc, const u16* __restrict__ vc,
                                                    const u16* __restrict__ w1, u16* __restrict__ o_out, u16* __restrict__ h_out
                                                    ) {
    __shared__ __attribute__((aligned(1024))) char smem[K2_LDS];
    const int bid = blockIdx.x;
    const int head = (bid & 7) * 4 + ((bid >> 3) & 3), qb = bid >> 5;
    const int wid = __builtin_amdgcn_readfirstlane(threadIdx.x >> 6);
    char LDSP* lds = (char LDSP*)smem;
    const char* Xw = (const char*)xn + (size_t)(qb * 8 + wid) * 64 * 1024;
    f32x16 accq[4][1];
    dg_zero_acc<false>(accq);
    K2Attn a;
    {
        DGState<K2Q> st; DGFrags<K2Q> f;
        dg_setup<K2Q>(st, Xw, 64, (const char*)(wq + head * 128), 4096, lds);
        dg_prologue<K2Q>(st, f);
        {
            const int lane = threadIdx.x & 63, kvh = head >> 3;
            a.kbase = (const char*)(kc + ((size_t)kvh * 2048 + 1536) * 128);
            a.vbase = (const char*)(vc + ((size_t)kvh * 2048 + 1536) * 128);
            a.lds = lds; a.lds_u = (unsigned)(size_t)lds; a.dst = wid * 2048;
#pragma unroll
            for (int i = 0; i < 2; ++i) {
                const int pc = wid * 2 + i;
                const int key = 4 * pc + (lane >> 4), c = (lane & 15) ^ (key & 15);
                a.koff[i] = key * 256 + c * 16;
                const int kv = 4 * pc + ((lane >> 2) & 3);
                a.voff[i] = kv * 256 + ((lane >> 4) * 32 + (lane & 3) * 8) * 2;
            }
        }
        dg_start<K2Q>(st, f);
        static_for<0, 16>([&](auto tc) {
            constexpr int t = decltype(tc)::value;
            constexpr auto hook = [](int u) { return (u >= 1 && u <= 5) ? 2 : 0; };
            dg_stage<K2Q, false, (t >= 12), (t & 3), hook(t - 1) + hook(t - 2)>(st, t, accq, f);
            if constexpr (t == 1) k2_issue_k(a, 0);
            if constexpr (t == 2) k2_issue_v(a, 0);
            if constexpr (t == 3) k2_issue_k(a, 1);
            if constexpr (t == 4) k2_issue_v(a, 1);
            if constexpr (t == 5) k2_issue_k(a, 2);
        });
        dg_drain_mfma<false>(accq);
    }
    bf16x8 qf[8];
#pragma unroll
    for (int u = 0; u < 8; ++u) {
        u32x4 w;
#pragma unroll
        for (int i = 0; i < 4; ++i) w[i] = pk_bf16(accq[u >> 1][0][8 * (u & 1) + 2 * i], accq[u >> 1][0][8 * (u & 1) + 2 * i + 1]);
        qf[u] = __builtin_bit_cast(bf16x8, w);
    }
    {
        int tz = threadIdx.x; asm volatile("" : "+v"(tz));
        const int lane = tz & 63, h = lane >> 5;
        a.k_addr = 4 * K2_TB + (lane & 31) * 256 + ((((2 * h) ^ (lane & 15)) & 15) << 4);
        const int gg = (lane >> 4) & 1, p = lane & 3, q4 = (lane & 15) >> 2;
        const int n4 = 4 * (p & 1) + 2 * gg + (p >> 1);
        const unsigned vl = h * 4 * 256 + q4 * 64 + n4 * 8;
        a.v_addrA = 6 * K2_TB + vl; a.v_addrB = 3 * K2_TB + vl;
        asm volatile("" : "+v"(a.k_addr), "+v"(a.v_addrA), "+v"(a.v_addrB));
    }
    K2Soft z;
#pragma unroll
    for (int t = 0; t < 4; ++t) z.o[t] = (f32x16){};
    z.m_run = -INFINITY; z.l_run = 0.f; z.alpha = 1.f; z.mc = 0.f; z.ps = 0.f;
    const bool hasF = head < 28;
    f32x4 accf[8][2];
    DGState<K2F> stf; DGFrags<K2F> ff;
    auto top = [&](auto ic) {
        constexpr int i = decltype(ic)::value;
        if constexpr (i >= 3) wait_vm<(i - 1 <= 5 ? 4 : (i - 1 == 6 ? 2 : 0))>();
        wg_barrier();
        if constexpr (i >= 1 && i <= 5) k2_issue_k(a, i + 2);
        if constexpr (i >= 1 && i <= 6) k2_issue_v(a, i + 1);
        if constexpr (i == 8) {
            if (hasF) {
                dg_setup<K2F>(stf, Xw, 64, (const char*)(w1 + head * 128), 3584, lds);
                dg_prologue<K2F>(stf, ff);
            }
        }
    };
#define K2_STAMP(n)
    if (wid < 4) {
        top(std::integral_constant<int, 0>{});
        static_for<0, 9>([&](auto jc) {
            constexpr int j = decltype(jc)::value;
            K2_STAMP(4 * j);
            if constexpr (j >= 1) top(jc);
            K2_STAMP(4 * j + 1);
            k2_phaseA<j>(a, qf, z);
            K2_STAMP(4 * j + 2);
            k2_phaseB<j>(a, z);
            K2_STAMP(4 * j + 3);
        });
    } else {
        top(std::integral_constant<int, 0>{});
        static_for<0, 9>([&](auto jc) {
            constexpr int j = decltype(jc)::value;
            K2_STAMP(4 * j + 1);
            k2_phaseA<j>(a, qf, z);
            K2_STAMP(4 * j + 2);
            if constexpr (j < 8) top(std::integral_constant<int, j + 1>{});
            K2_STAMP(4 * j);
            k2_phaseB<j>(a, z);
            K2_STAMP(4 * j + 3);
        });
    }
    {
        int tz = threadIdx.x; asm volatile("" : "+v"(tz));
        const int lane = tz & 63;
        auto rr = __builtin_amdgcn_permlane32_swap(__float_as_uint(z.l_run), __float_as_uint(z.l_run), false, false);
        const float inv = 1.0f / (__uint_as_float(rr[0]) + __uint_as_float(rr[1]));
        char* base = (char*)o_out + ((size_t)(qb * 8 + wid) * 256 + head * 8) * 1024;
#pragma unroll
        for (int t = 0; t < 4; ++t) {
#pragma unroll
            for (int r = 0; r < 16; ++r) z.o[t][r] *= inv;
            store_tile_frag<false>(base + t * 2048, z.o[t], lane, 0);
        }
    }
    if (hasF) {
        dg_zero_acc<false>(accf);
        dg_start<K2F>(stf, ff);
        dg_group<K2F, false, false>(stf, 0, accf, ff);
        dg_group<K2F, false, false>(stf, 4, accf, ff);
        dg_group<K2F, false, false>(stf, 8, accf, ff);
        dg_group<K2F, false, true>(stf, 12, accf, ff);
        dg_drain_mfma<false>(accf);
        int tz = threadIdx.x; asm volatile("" : "+v"(tz));
        const int lane = tz & 63;
        char* base = (char*)h_out + ((size_t)(qb * 8 + wid) * 224 + head * 8) * 1024;
#pragma unroll
        for (int x = 0; x < 8; ++x)
#pragma unroll
            for (int y = 0; y < 2; ++y) store_tile_frag<true>(base + x * 1024, accf[x][y], lane, y);
    }
}

using K3C = DC<128, 128, 4, 4, 16>;
constexpr int K3_STG_PITCH = 272;
constexpr int K3_STG_WAVE = 32 * K3_STG_PITCH;
constexpr int K3_LDS = K3C::LDS_BYTES + 4 * K3_STG_WAVE + 16;

typedef __attribute__((address_space(1))) unsigned gu32;
DI unsigned atomic_load_agent(unsigned* p) { return __hip_atomic_load(p, __ATOMIC_RELAXED, __HIP_MEMORY_SCOPE_AGENT); }
DI unsigned atomic_add_agent(unsigned* p, unsigned v) { return __hip_atomic_fetch_add(p, v, __ATOMIC_RELAXED, __HIP_MEMORY_SCOPE_AGENT); }

DI float silu_bf(float a) {
    const float x = rbf(a);
    const float e = __builtin_amdgcn_exp2f(-x * 1.4426950408889634f);
    return rbf(x * __builtin_amdgcn_rcpf(1.0f + e));
}

DI bool k3_exchange(const u32x4 (&mine)[8], u32x4 (&theirs)[8], unsigned* cnt, char* my_slot, char* their_slot, int lane, int wid, volatile unsigned LDSP* word) {
    if (wid == 0) {
        const unsigned c = atomic_load_agent(cnt);
        if (lane == 0) *word = c;
    }
    __syncthreads();
    unsigned c = __builtin_amdgcn_readfirstlane(*word);
    if (c == 0) {
        const __amdgpu_buffer_rsrc_t rs = __builtin_amdgcn_make_buffer_rsrc(my_slot, 0, 8192u, 0x00020000);
#pragma unroll
        for (int i = 0; i < 8; ++i) __builtin_amdgcn_raw_buffer_store_b128(mine[i], rs, i * 1024 + lane * 16, 0, 16  );
        asm volatile("s_waitcnt vmcnt(0)" ::: "memory");
        __syncthreads();
        if (wid == 0) {
            unsigned old = 0;
            if (lane == 0) old = atomic_add_agent(cnt, 1u);
            if (lane == 0) *word = old;
        }
        __syncthreads();
        c = __builtin_amdgcn_readfirstlane(*word);
        if (c == 0) return false;
    }
    const __amdgpu_buffer_rsrc_t rt = __builtin_amdgcn_make_buffer_rsrc(their_slot, 0, 8192u, 0x00020000);
#pragma unroll
    for (int i = 0; i < 8; ++i) theirs[i] = __builtin_amdgcn_raw_buffer_load_b128(rt, i * 1024 + lane * 16, 0, 16  );
    return true;
}

__global__ __launch_bounds__(256, 1) void k3_kernel(const u16* __restrict__ o_f, const u16* __restrict__ wo, const u16* __restrict__ h_f,
                                                    const u16* __restrict__ w2, const u16* __restrict__ x, u16* __restrict__ out,
                                                    char* __restrict__ exs, char* __restrict__ exg, unsigned* __restrict__ cnts
                                                    ) {
    using C = K3C;
    __shared__ __attribute__((aligned(1024))) char smem[K3_LDS];
    const int bid = blockIdx.x, xcd = bid & 7, i5 = bid >> 3;
    const int type = xcd >> 2, pm = (xcd & 3) * 4 + (i5 & 3), pn = i5 >> 2;
    const int tile = pm * 8 + pn;
    const int wid = __builtin_amdgcn_readfirstlane(threadIdx.x >> 6);
    f32x4 acc[C::TN][C::TM];
    dg_zero_acc<true>(acc);
    const int Kd = type == 0 ? 4096 : 3584;
    {
        const char* Xw = (const char*)(type == 0 ? o_f : h_f) + (size_t)(pm * 4 + wid) * (Kd / 16) * 1024;
        const u16* W = (type == 0 ? wo : w2) + pn * 128;
        DGState<C> st; DGFrags<C> f;
        dg_setup<C>(st, Xw, Kd / 16, (const char*)W, 1024, (char LDSP*)smem);
        dg_prologue<C>(st, f);
        dg_run<C, true>(st, Kd / 64, acc, f);
    }
    int tz = threadIdx.x; asm volatile("" : "+v"(tz));
    const int lane = tz & 63;
    const size_t row0 = (size_t)pm * 128 + wid * 32;
    const u16* xp = x + (row0 + (lane >> 4)) * 1024 + pn * 128 + (lane & 15) * 8;
    u32x4 xv[8];
#pragma unroll
    for (int i = 0; i < 8; ++i) xv[i] = *(const u32x4*)(xp + (size_t)i * 4 * 1024);
    u32x4 mine[8], theirs[8];
#pragma unroll
    for (int i = 0; i < 8; ++i) {
        if (type == 0) mine[i] = (u32x4){pk_bf16(acc[i][0][0], acc[i][0][1]), pk_bf16(acc[i][0][2], acc[i][0][3]), pk_bf16(acc[i][1][0], acc[i][1][1]), pk_bf16(acc[i][1][2], acc[i][1][3])};
        else mine[i] = (u32x4){pk_bf16(silu_bf(acc[i][0][0]), silu_bf(acc[i][0][1])), pk_bf16(silu_bf(acc[i][0][2]), silu_bf(acc[i][0][3])),
                               pk_bf16(silu_bf(acc[i][1][0]), silu_bf(acc[i][1][1])), pk_bf16(silu_bf(acc[i][1][2]), silu_bf(acc[i][1][3]))};
    }
    const size_t slot = ((size_t)tile * 4 + wid) * 8192;
    if (!k3_exchange(mine, theirs, cnts + tile * 4, (type == 0 ? exs : exg) + slot, (type == 0 ? exg : exs) + slot, lane, wid,
                     (volatile unsigned LDSP*)((char LDSP*)smem + K3_LDS - 16))) {
        return;
    }
    char LDSP* stg = (char LDSP*)smem + C::LDS_BYTES + wid * K3_STG_WAVE;
#pragma unroll
    for (int i = 0; i < 8; ++i)
#pragma unroll
        for (int tb = 0; tb < 2; ++tb) {
            const unsigned a0 = mine[i][2 * tb], a1 = mine[i][2 * tb + 1], b0 = theirs[i][2 * tb], b1 = theirs[i][2 * tb + 1];
            const float p0 = __uint_as_float(a0 << 16) * __uint_as_float(b0 << 16), p1 = __uint_as_float(a0 & 0xffff0000u) * __uint_as_float(b0 & 0xffff0000u);
            const float p2 = __uint_as_float(a1 << 16) * __uint_as_float(b1 << 16), p3 = __uint_as_float(a1 & 0xffff0000u) * __uint_as_float(b1 & 0xffff0000u);
            *(u32x2 LDSP*)(stg + (16 * tb + (lane & 15)) * K3_STG_PITCH + (16 * i + 4 * (lane >> 4)) * 2) = (u32x2){pk_bf16(p0, p1), pk_bf16(p2, p3)};
        }
    u16* op = out + (row0 + (lane >> 4)) * 1024 + pn * 128 + (lane & 15) * 8;
#pragma unroll
    for (int i = 0; i < 8; ++i) {
        const u32x4 pv = *(const u32x4 LDSP*)(stg + (4 * i + (lane >> 4)) * K3_STG_PITCH + (lane & 15) * 16);
        u32x4 ov;
#pragma unroll
        for (int e = 0; e < 4; ++e)
            ov[e] = pk_bf16(__uint_as_float(xv[i][e] << 16) + __uint_as_float(pv[e] << 16), __uint_as_float(xv[i][e] & 0xffff0000u) + __uint_as_float(pv[e] & 0xffff0000u));
        *(u32x4*)(op + (size_t)i * 4 * 1024) = ov;
    }
}

constexpr int M_ = 2048, D_ = 1024;

__global__ __launch_bounds__(256) void ln_kernel(const u16* __restrict__ x, u16* __restrict__ xn, unsigned* __restrict__ cnts) {
    const int lane = threadIdx.x & 63, wid = threadIdx.x >> 6;
    if (blockIdx.x == 0 && threadIdx.x < 128) cnts[threadIdx.x * 4] = 0u;
    const int row = blockIdx.x * 4 + wid;
    const u32x4* src = (const u32x4*)(x + (size_t)row * D_ + lane * 16);
    u32x4 a = src[0], b = src[1];
    float v[16];
#pragma unroll
    for (int i = 0; i < 4; ++i) { v[2 * i] = __uint_as_float(a[i] << 16); v[2 * i + 1] = __uint_as_float(a[i] & 0xffff0000u);
                                  v[8 + 2 * i] = __uint_as_float(b[i] << 16); v[8 + 2 * i + 1] = __uint_as_float(b[i] & 0xffff0000u); }
    float s = 0.f;
#pragma unroll
    for (int i = 0; i < 16; ++i) s += v[i];
#pragma unroll
    for (int o = 32; o >= 1; o >>= 1) s += __shfl_xor(s, o);
    const float mean = s * (1.0f / 1024.0f);
    float q = 0.f;
#pragma unroll
    for (int i = 0; i < 16; ++i) { const float d = v[i] - mean; q += d * d; }
#pragma unroll
    for (int o = 32; o >= 1; o >>= 1) q += __shfl_xor(q, o);
    const float var = q * (1.0f / 1024.0f);
    const float mb = rbf(mean), vb = rbf(var);
    const float den = rbf(sqrtf(rbf(vb + 1e-5f)));
    unsigned o[8];
#pragma unroll
    for (int i = 0; i < 8; ++i) {
        const float d0 = rbf(v[2 * i] - mb), d1 = rbf(v[2 * i + 1] - mb);
        o[i] = pk_bf16(d0 / den, d1 / den);
    }
    char* dst = (char*)xn + ((size_t)(row >> 5) * (D_ / 16) + lane) * 1024 + (row & 31) * 16;
    *(u32x4*)dst = (u32x4){o[0], o[1], o[2], o[3]};
    *(u32x4*)(dst + 512) = (u32x4){o[4], o[5], o[6], o[7]};
}

constexpr size_t WS_XN = 0, WS_H = 4u << 20, WS_O = 20u << 20, WS_EXS = 36u << 20, WS_EXG = 40u << 20, WS_CNT = 44u << 20;

extern "C" void kernel_launch(void* const* d_in, const int* in_sizes, int n_in, void* d_out, int out_size, void* d_ws, size_t ws_size,
                              hipStream_t stream) {
    const u16* x = (const u16*)d_in[0];
    const u16* kc = (const u16*)d_in[1];
    const u16* vc = (const u16*)d_in[2];
    const u16* wq = (const u16*)d_in[3];
    const u16* wo = (const u16*)d_in[4];
    const u16* w1 = (const u16*)d_in[6];
    const u16* w2 = (const u16*)d_in[7];
    char* ws = (char*)d_ws;
    u16* xn = (u16*)(ws + WS_XN); u16* h = (u16*)(ws + WS_H); u16* o = (u16*)(ws + WS_O);
    unsigned* cnts = (unsigned*)(ws + WS_CNT);
    ln_kernel<<<512, 256, 0, stream>>>(x, xn, cnts);
    k2_kernel<<<256, 512, 0, stream>>>(xn, wq, kc, vc, w1, o, h);
    k3_kernel<<<256, 256, 0, stream>>>(o, wo, h, w2, x, (u16*)d_out, ws + WS_EXS, ws + WS_EXG, cnts);
}
extern "C" void launch_one(int which, void* const* d_in, void* d_out, void* d_ws, hipStream_t stream) {
    const u16* x = (const u16*)d_in[0];
    const u16* kc = (const u16*)d_in[1];
    const u16* vc = (const u16*)d_in[2];
    const u16* wq = (const u16*)d_in[3];
    const u16* wo = (const u16*)d_in[4];
    const u16* w1 = (const u16*)d_in[6];
    const u16* w2 = (const u16*)d_in[7];
    char* ws = (char*)d_ws;
    u16* xn = (u16*)(ws + WS_XN); u16* h = (u16*)(ws + WS_H); u16* o = (u16*)(ws + WS_O);
    unsigned* cnts = (unsigned*)(ws + WS_CNT);
    if (which == 0) ln_kernel<<<512, 256, 0, stream>>>(x, xn, cnts);
    if (which == 1) k2_kernel<<<256, 512, 0, stream>>>(xn, wq, kc, vc, w1, o, h);
    if (which == 2) { ln_kernel<<<1, 256, 0, stream>>>(x, xn, cnts); k3_kernel<<<256, 256, 0, stream>>>(o, wo, h, w2, x, (u16*)d_out, ws + WS_EXS, ws + WS_EXG, cnts); }
}
```

```cpp
#include <hip/hip_runtime.h>
#include <stdint.h>

typedef short bf16x8 __attribute__((ext_vector_type(8)));
typedef short s16x4 __attribute__((ext_vector_type(4)));
typedef float f32x16 __attribute__((ext_vector_type(16)));
typedef float f32x2 __attribute__((ext_vector_type(2)));
typedef unsigned u32x4 __attribute__((ext_vector_type(4)));
typedef unsigned short u16;
typedef __bf16 bf16x2_t __attribute__((ext_vector_type(2)));
#define LDSP __attribute__((address_space(3)))
#define DI __device__ __forceinline__

DI float bf2f(u16 v) { return __uint_as_float(((unsigned)v) << 16); }
DI unsigned pk_bf16(float lo, float hi) {
    f32x2 f = {lo, hi};
    bf16x2_t b = __builtin_convertvector(f, bf16x2_t);
    return __builtin_bit_cast(unsigned, b);
}
DI float rbf(float x) {
    unsigned p = pk_bf16(x, x);
    return __uint_as_float(p << 16);
}
DI s16x4 tr_read(const char LDSP* p) { return __builtin_amdgcn_ds_read_tr16_b64_v4i16((s16x4 LDSP*)p); }
DI bf16x8 cat8(s16x4 lo, s16x4 hi) { return __builtin_shufflevector(lo, hi, 0, 1, 2, 3, 4, 5, 6, 7); }
#define MFMA32(a, b, c) __builtin_amdgcn_mfma_f32_32x32x16_bf16((a), (b), (c), 0, 0, 0)

#include <type_traits>
template <int I, int N, class F>
DI void static_for(F&& f) {
    if constexpr (I < N) {
        f(std::integral_constant<int, I>{});
        static_for<I + 1, N>(f);
    }
}
typedef float f32x4 __attribute__((ext_vector_type(4)));
#define MFMA16(a, b, c) __builtin_amdgcn_mfma_f32_16x16x32_bf16((a), (b), (c), 0, 0, 0)

template <int BM_, int BN_, int BK_, int NWM_, int NWN_, int S_, int SH_, int ABL_ = 0>
struct GC {
    static constexpr int ABL = ABL_;
    static constexpr int BM = BM_, BN = BN_, BK = BK_, NWM = NWM_, NWN = NWN_, S = S_, SH = SH_;
    static constexpr int NW = NWM * NWN, NT = NW * 64;
    static constexpr int TT = SH, KK = SH == 32 ? 16 : 32;
    static constexpr int TM = BM / TT / NWM, TN = BN / TT / NWN, KS = BK / KK, NTL = BN / 32, KS16 = BK / 16;
    static constexpr int XB = BM * BK * 2, WB = BN * BK * 2, SB = XB + WB;
    static constexpr int XP = XB / 1024, WP = WB / 1024, XPW = XP / NW, WPW = WP / NW, L = XPW + WPW;
    static constexpr int LDS_BYTES = S * SB;
    static constexpr int NM = TN * TM, NR = 2 * TN + TM;
    static_assert(XP % NW == 0 && WP % NW == 0, "pieces per wave");
    static_assert((S * KS) % 2 == 0, "fragment double buffer parity");
    static_assert(BN % 128 == 0, "BN");
    static_assert(SH == 32 || (TM % 2 == 0 && TN % 2 == 0), "TM/TN even for the 16x16 shape");
};
template <int SH> struct AccT;
template <> struct AccT<32> { typedef f32x16 t; };
template <> struct AccT<16> { typedef f32x4 t; };

template <class C>
struct GemmState {
    const char* xsrc[C::XPW];
    const char* wsrc[C::WPW];
    long xstride, wstride;
    char LDSP* lds;
    unsigned xdst, wdst;
    unsigned a_off[2], b_off;
};

template <int N> DI void wait_vm_lgkm0() { asm volatile("s_waitcnt vmcnt(%0) lgkmcnt(0)" ::"n"(N) : "memory"); }
DI void wg_barrier() {
    asm volatile("" ::: "memory");
    __builtin_amdgcn_s_barrier();
    asm volatile("" ::: "memory");
}

template <class C>
struct GemmFrags {
    s16x4 alo[2][C::TN], ahi[2][C::TN];
    bf16x8 b[2][C::TM];
};
template <class C>
DI void gemm_read_op(const GemmState<C>& st, int k, int buf, int ks, int sel, GemmFrags<C>& f) {
    if (k >= 2 && k < 2 + C::TM) {
        const int tb = k - 2;
        if constexpr (C::SH == 32)
            f.b[sel][tb] = *(const bf16x8 LDSP*)(st.lds + (buf * C::SB + (tb * C::KS16 + ks) * 1024) + st.b_off);
        else
            f.b[sel][tb] = *(const bf16x8 LDSP*)(st.lds + (buf * C::SB + ((tb >> 1) * C::KS16 + 2 * ks) * 1024 + (tb & 1) * 256) + st.b_off);
    } else {
        const int idx = k < 2 ? k : k - C::TM, ta = idx >> 1, half = idx & 1;
        const char LDSP* p;
        if constexpr (C::SH == 32) p = st.lds + (buf * C::SB + C::XB + ((4 * ks + half) * C::NTL + ta) * 256) + st.a_off[0];
        else p = st.lds + (buf * C::SB + C::XB + ((8 * ks + half) * C::NTL + (ta >> 1)) * 256) + st.a_off[ta & 1];
        if (half) f.ahi[sel][ta] = tr_read(p); else f.alo[sel][ta] = tr_read(p);
    }
}
template <class C>
DI void gemm_glds_op(const GemmState<C>& st, int g, int t, int buf) {
    if (g < C::XPW)
        __builtin_amdgcn_global_load_lds((const unsigned*)(st.xsrc[g] + (long)t * st.xstride),
                                         (unsigned LDSP*)(st.lds + buf * C::SB + st.xdst + g * 1024), 16, 0, 0);
    else
        __builtin_amdgcn_global_load_lds((const unsigned*)(st.wsrc[g - C::XPW] + (long)t * st.wstride),
                                         (unsigned LDSP*)(st.lds + buf * C::SB + C::XB + st.wdst + (g - C::XPW) * 1024), 16, 0, 0);
}
template <class C> constexpr int read_slot(int k) {
    constexpr int SPAN = C::NM - (C::NM / 4 > 1 ? C::NM / 4 : 1);
    return k * SPAN / C::NR;
}
template <class C> constexpr int glds_slot(int g) {
    constexpr int OFF = C::NM / 4;
    return OFF + g * (C::NM - OFF) / C::L;
}

template <class C, int B, int KSI, bool TAIL>
DI void gemm_step(const GemmState<C>& st, int t, typename AccT<C::SH>::t (&acc)[C::TN][C::TM], GemmFrags<C>& f) {
    constexpr int cur = (C::ABL & 1) ? 0 : ((B * C::KS + KSI) & 1), nxt = cur ^ 1;
    constexpr bool LAST = KSI == C::KS - 1;
    constexpr bool HAS_NEXT = !(LAST && TAIL && B == C::S - 1);
    constexpr bool SYNC = LAST && HAS_NEXT, ISSUE = SYNC && !TAIL;
    constexpr int nbuf = LAST ? (B + 1) % C::S : B, nks = LAST ? 0 : KSI + 1;
    if constexpr (SYNC) {
        constexpr int inflight = TAIL ? (C::S - B - 2) : (C::S - 2);
        wait_vm_lgkm0<inflight * C::L>();
        wg_barrier();
    }
    static_for<0, C::NM>([&](auto ic) {
        constexpr int i = decltype(ic)::value, ta = i / C::TM, tb = i % C::TM;
        if constexpr (C::ABL & 4) { asm volatile("" :: "v"(f.alo[cur][ta]), "v"(f.ahi[cur][ta]), "v"(f.b[cur][tb])); }
        else if constexpr (C::SH == 32) acc[ta][tb] = MFMA32(cat8(f.alo[cur][ta], f.ahi[cur][ta]), f.b[cur][tb], acc[ta][tb]);
        else acc[ta][tb] = MFMA16(cat8(f.alo[cur][ta], f.ahi[cur][ta]), f.b[cur][tb], acc[ta][tb]);
        if constexpr (HAS_NEXT && !(C::ABL & 1)) {
            static_for<0, C::NR>([&](auto kc) {
                constexpr int k = decltype(kc)::value;
                if constexpr (read_slot<C>(k) == i) gemm_read_op<C>(st, k, nbuf, nks, nxt, f);
            });
        }
        if constexpr (ISSUE && !(C::ABL & 2)) {
            static_for<0, C::L>([&](auto gc) {
                constexpr int g = decltype(gc)::value;
                if constexpr (glds_slot<C>(g) == i) gemm_glds_op<C>(st, g, t + C::S, B);
            });
        }
        __builtin_amdgcn_sched_barrier(0);
    });
}

template <class C, int B, bool TAIL, int KSI = 0>
DI void gemm_stage_body(const GemmState<C>& st, int t, typename AccT<C::SH>::t (&acc)[C::TN][C::TM], GemmFrags<C>& f) {
    gemm_step<C, B, KSI, TAIL>(st, t, acc, f);
    if constexpr (KSI + 1 < C::KS) gemm_stage_body<C, B, TAIL, KSI + 1>(st, t, acc, f);
}
template <class C, bool TAIL, int B = 0>
DI void gemm_group(const GemmState<C>& st, int t0, typename AccT<C::SH>::t (&acc)[C::TN][C::TM], GemmFrags<C>& f) {
    gemm_stage_body<C, B, TAIL>(st, t0 + B, acc, f);
    if constexpr (B + 1 < C::S) gemm_group<C, TAIL, B + 1>(st, t0, acc, f);
}
template <class C, int B = 0>
DI void gemm_prologue_issue(const GemmState<C>& st) {
#pragma unroll
    for (int g = 0; g < C::L; ++g) gemm_glds_op<C>(st, g, B, B);
    if constexpr (B + 1 < C::S) gemm_prologue_issue<C, B + 1>(st);
}

template <class C>
DI void gemm_core(const char* __restrict__ Xg, int xks, const char* __restrict__ Wg, int ldw, int nt, char* smem,
                  typename AccT<C::SH>::t (&acc)[C::TN][C::TM], unsigned long long& T1) {
    const int tid = threadIdx.x, lane = tid & 63;
    const int wid = __builtin_amdgcn_readfirstlane(tid >> 6);
    const int wm = wid / C::NWN, wn = wid % C::NWN;
    GemmState<C> st;
    st.lds = (char LDSP*)smem;
#pragma unroll
    for (int i = 0; i < C::XPW; ++i) {
        const int pc = wid * C::XPW + i, mt = pc / C::KS16, ks = pc % C::KS16;
        st.xsrc[i] = Xg + ((long)mt * xks + ks) * 1024 + lane * 16;
    }
    {
        const int q4 = (lane >> 2) & 3;
#pragma unroll
        for (int j = 0; j < C::WPW; ++j) {
            const int pc = wid * C::WPW + j, kq = pc / (C::BN / 128), a = pc % (C::BN / 128);
            const int cc = C::SH == 32 ? (lane & 3) : ((lane & 3) ^ (2 * ((kq >> 1) & 1)));
            st.wsrc[j] = Wg + ((long)(4 * kq + q4) * ldw + 128 * a + 32 * (lane >> 4) + 8 * cc) * 2;
        }
    }
    st.xstride = (long)C::KS16 * 1024;
    st.wstride = (long)C::BK * ldw * 2;
    st.xdst = wid * C::XPW * 1024;
    st.wdst = wid * C::WPW * 1024;
    if constexpr (C::SH == 32) {
        const int h = lane >> 5, gg = (lane >> 4) & 1, p = lane & 3, q4 = (lane & 15) >> 2;
        const int n4 = 4 * (p & 1) + 2 * gg + (p >> 1);
        st.a_off[0] = (2 * h * C::NTL) * 256 + q4 * 64 + n4 * 8 + wn * C::TN * 256;
        st.a_off[1] = st.a_off[0];
        st.b_off = lane * 16 + wm * C::TM * C::KS16 * 1024;
    } else {
        const int g = lane >> 4, p = lane & 3, q4 = (lane & 15) >> 2;
        const int base = (2 * g * C::NTL) * 256 + q4 * 64 + p * 8 + wn * (C::TN / 2) * 256;
        st.a_off[0] = base + (g & 1) * 32;
        st.a_off[1] = base + ((g & 1) ^ 1) * 32;
        st.b_off = (g >> 1) * 1024 + (g & 1) * 512 + (lane & 15) * 16 + wm * (C::TM / 2) * C::KS16 * 1024;
    }
    GemmFrags<C> f;
    gemm_prologue_issue<C>(st);
    wait_vm_lgkm0<(C::S - 1) * C::L>();
    wg_barrier();
    T1 = __builtin_amdgcn_s_memtime();
#pragma unroll
    for (int k = 0; k < C::NR; ++k) gemm_read_op<C>(st, k, 0, 0, 0, f);
    __builtin_amdgcn_sched_barrier(0);
    int t0 = 0;
    for (; t0 < nt - C::S; t0 += C::S) gemm_group<C, false>(st, t0, acc, f);
    gemm_group<C, true>(st, t0, acc, f);
}

template <bool RELU>
DI void store_tile_frag(char* base, const f32x16& a, int lane, int  ) {
    const int h = lane >> 5;
    char* p = base + h * 1024 + (lane & 31) * 16;
    unsigned w[8];
#pragma unroll
    for (int i = 0; i < 8; ++i) {
        float lo = a[2 * i], hi = a[2 * i + 1];
        if (RELU) { lo = fmaxf(lo, 0.f); hi = fmaxf(hi, 0.f); }
        w[i] = pk_bf16(lo, hi);
    }
    *(u32x4*)p = (u32x4){w[0], w[1], w[2], w[3]};
    *(u32x4*)(p + 512) = (u32x4){w[4], w[5], w[6], w[7]};
}
DI void store_tile_frag_wt(__amdgpu_buffer_rsrc_t rs, unsigned base_off, const f32x16& a, int lane) {
    const unsigned p = base_off + (lane >> 5) * 1024 + (lane & 31) * 16;
    u32x4 w0, w1;
#pragma unroll
    for (int i = 0; i < 4; ++i) { w0[i] = pk_bf16(a[2 * i], a[2 * i + 1]); w1[i] = pk_bf16(a[8 + 2 * i], a[8 + 2 * i + 1]); }
    __builtin_amdgcn_raw_buffer_store_b128(w0, rs, p, 0, 16  );
    __builtin_amdgcn_raw_buffer_store_b128(w1, rs, p + 512, 0, 16  );
}
typedef unsigned u32x2 __attribute__((ext_vector_type(2)));
template <bool RELU>
DI void store_tile_frag(char* base, const f32x4& a, int lane, int mhalf) {
    const int g = lane >> 4;
    char* p = base + (g >> 1) * 512 + (16 * mhalf + (lane & 15)) * 16 + (g & 1) * 8;
    float v0 = a[0], v1 = a[1], v2 = a[2], v3 = a[3];
    if (RELU) { v0 = fmaxf(v0, 0.f); v1 = fmaxf(v1, 0.f); v2 = fmaxf(v2, 0.f); v3 = fmaxf(v3, 0.f); }
    *(u32x2*)p = (u32x2){pk_bf16(v0, v1), pk_bf16(v2, v3)};
}

template <int BM_, int BN_, int NW_, int S_, int SH_>
struct DC {
    static constexpr int BM = BM_, BN = BN_, NW = NW_, S = S_, SH = SH_, BK = 64;
    static constexpr int NT = NW * 64;
    static constexpr int TT = SH, KK = SH == 32 ? 16 : 32;
    static constexpr int TW = BM / NW, TM = TW / TT, TN = BN / TT, KS = BK / KK, NTL = BN / 32;
    static constexpr int TM32 = TW / 32;
    static constexpr int WB = BN * BK * 2, WP = WB / 1024, LW = WP / NW;
    static constexpr int LB = TM * KS;
    static constexpr int LG = LW + LB;
    static constexpr int LDS_BYTES = S * WB;
    static constexpr int NM = TN * TM;
    static_assert(WP % NW == 0, "W pieces per wave");
    static_assert(BN % 128 == 0 && TW % 32 == 0, "shape");
    static_assert(SH == 32 || TN % 2 == 0, "TN even for the 16x16 shape");
    static_assert((S - 2) * LG <= 63, "vmcnt range");
};

template <class C>
struct DGState {
    const char* wbase;
    unsigned woff[C::LW];
    const char* xbase;
    unsigned xoff[C::TM32];
    long wstride;
    char LDSP* lds;
    unsigned lds_u;
    unsigned wdst;
    unsigned a_off[2];
};
template <class C>
struct DGFrags {
    s16x4 alo[2][C::TN], ahi[2][C::TN];
    bf16x8 b[C::S][C::KS][C::TM];
};

DI void glds16_asm(unsigned lds_addr, const char* sbase, unsigned voff) {
    asm volatile("s_mov_b32 m0, %0\n\ts_nop 0\n\tglobal_load_lds_dwordx4 %1, %2" :: "s"(lds_addr), "v"(voff), "s"(sbase) : "memory");
}
template <class C>
DI void dg_glds(const DGState<C>& st, int g, int t, int slot) {
    glds16_asm(st.lds_u + slot * C::WB + st.wdst + g * 1024, st.wbase + (long)t * st.wstride, st.woff[g]);
}
template <bool AG> DI void mfma_asm(f32x4& acc, const bf16x8& a, const bf16x8& b) {
    if constexpr (AG) asm volatile("v_mfma_f32_16x16x32_bf16 %0, %1, %2, %0" : "+a"(acc) : "v"(a), "v"(b));
    else asm volatile("v_mfma_f32_16x16x32_bf16 %0, %1, %2, %0" : "+v"(acc) : "v"(a), "v"(b));
}
template <bool AG> DI void mfma_asm(f32x16& acc, const bf16x8& a, const bf16x8& b) {
    if constexpr (AG) asm volatile("v_mfma_f32_32x32x16_bf16 %0, %1, %2, %0" : "+a"(acc) : "v"(a), "v"(b));
    else asm volatile("v_mfma_f32_32x32x16_bf16 %0, %1, %2, %0" : "+v"(acc) : "v"(a), "v"(b));
}
template <bool AG, class T, int TN, int TM>
DI void dg_zero_acc(T (&acc)[TN][TM]) {
#pragma unroll
    for (int a = 0; a < TN; ++a)
#pragma unroll
        for (int b = 0; b < TM; ++b) {
            acc[a][b] = (T){};
            if constexpr (AG) asm volatile("" : "+a"(acc[a][b]));
            else asm volatile("" : "+v"(acc[a][b]));
        }
    asm volatile("s_nop 1");
}
template <bool AG, class T, int TN, int TM>
DI void dg_drain_mfma(T (&acc)[TN][TM]) {
    asm volatile("s_nop 15");
#pragma unroll
    for (int a = 0; a < TN; ++a)
#pragma unroll
        for (int b = 0; b < TM; ++b) {
            if constexpr (AG) asm volatile("" : "+a"(acc[a][b]));
            else asm volatile("" : "+v"(acc[a][b]));
        }
    __builtin_amdgcn_sched_barrier(0);
}
template <int IMM>
DI void gload16_asm(bf16x8& dst, const char* sbase, unsigned voff) {
    asm volatile("global_load_dwordx4 %0, %1, %2 offset:%3" : "=v"(dst) : "v"(voff), "s"(sbase), "n"(IMM) : "memory");
}
template <class C, int BUF, int D>
DI void dg_bload(const DGState<C>& st, int t, DGFrags<C>& f) {
    constexpr int ks = D / C::TM, tb = D % C::TM;
    const char* sb = st.xbase + (long)t * 4096;
    if constexpr (C::SH == 32) gload16_asm<ks * 1024>(f.b[BUF][ks][tb], sb, st.xoff[tb]);
    else gload16_asm<(tb & 1) * 256 + ks * 2048>(f.b[BUF][ks][tb], sb, st.xoff[tb >> 1]);
}
template <class C>
DI void dg_aread(const DGState<C>& st, int k, int slot, int ks, int sel, DGFrags<C>& f) {
    const int ta = k >> 1, half = k & 1;
    const char LDSP* p;
    if constexpr (C::SH == 32) p = st.lds + (slot * C::WB + ((4 * ks + half) * C::NTL + ta) * 256) + st.a_off[0];
    else p = st.lds + (slot * C::WB + ((8 * ks + half) * C::NTL + (ta >> 1)) * 256) + st.a_off[ta & 1];
    if (half) f.ahi[sel][ta] = tr_read(p); else f.alo[sel][ta] = tr_read(p);
}
#define DG_DRAIN 0
template <class C, int N0, int BUF>
DI void dg_wait(DGFrags<C>& f) {
    constexpr int N = DG_DRAIN ? 0 : N0;
    if constexpr (C::LB == 4) {
        constexpr int T = C::TM;
        asm volatile("s_waitcnt vmcnt(%4) lgkmcnt(0)"
                     : "+v"(f.b[BUF][0 / T][0 % T]), "+v"(f.b[BUF][1 / T][1 % T]), "+v"(f.b[BUF][2 / T][2 % T]), "+v"(f.b[BUF][3 / T][3 % T])
                     : "n"(N) : "memory");
    } else {
        static_assert(C::LB == 8, "LB");
        constexpr int T = C::TM;
        asm volatile("s_waitcnt vmcnt(%8) lgkmcnt(0)"
                     : "+v"(f.b[BUF][0 / T][0 % T]), "+v"(f.b[BUF][1 / T][1 % T]), "+v"(f.b[BUF][2 / T][2 % T]), "+v"(f.b[BUF][3 / T][3 % T]),
                       "+v"(f.b[BUF][4 / T][4 % T]), "+v"(f.b[BUF][5 / T][5 % T]), "+v"(f.b[BUF][6 / T][6 % T]), "+v"(f.b[BUF][7 / T][7 % T])
                     : "n"(N) : "memory");
    }
}
template <class C> constexpr int dg_aslot(int k) {
    return k * (C::NM - (C::NM / 4 > 1 ? C::NM / 4 : 1)) / (2 * C::TN);
}

template <class C, bool AG, bool TAIL, int B, int KSI, int EXTRA = 0>
DI void dg_step(const DGState<C>& st, int t, typename AccT<C::SH>::t (&acc)[C::TN][C::TM], DGFrags<C>& f) {
    constexpr int cur = (B * C::KS + KSI) & 1, nxt = cur ^ 1;
    constexpr bool LAST = KSI == C::KS - 1, FIRST = KSI == 0;
    constexpr bool HAS_NEXT = !(LAST && TAIL && B == C::S - 1);
    constexpr int nslot = LAST ? (B + 1) % C::S : B, nks = LAST ? 0 : KSI + 1;
    constexpr bool HAS_P = FIRST && (!TAIL || B == 0);
    constexpr int pslot = (B + C::S - 1) % C::S;
    if constexpr (LAST && HAS_NEXT) {
        dg_wait<C, (TAIL ? C::S - B - 2 : C::S - 2) * C::LG + EXTRA, (B + 1) % C::S>(f);
        wg_barrier();
    }
    static_for<0, C::NM>([&](auto ic) {
        constexpr int i = decltype(ic)::value, ta = i / C::TM, tb = i % C::TM;
        mfma_asm<AG>(acc[ta][tb], cat8(f.alo[cur][ta], f.ahi[cur][ta]), f.b[B][KSI][tb]);
        if constexpr (HAS_NEXT) {
            static_for<0, 2 * C::TN>([&](auto kc) {
                constexpr int k = decltype(kc)::value;
                if constexpr (dg_aslot<C>(k) == i) dg_aread<C>(st, k, nslot, nks, nxt, f);
            });
        }
        if constexpr (HAS_P) {
            static_for<0, C::LB>([&](auto dc) {
                constexpr int d = decltype(dc)::value;
                if constexpr (d * C::NM / C::LG == i) dg_bload<C, pslot, d>(st, t + C::S - 1, f);
            });
            static_for<0, C::LW>([&](auto gc) {
                constexpr int g = decltype(gc)::value;
                if constexpr ((C::LB + g) * C::NM / C::LG == i) dg_glds<C>(st, g, t + C::S - 1, pslot);
            });
        }
        __builtin_amdgcn_sched_barrier(0);
    });
}
template <class C, bool AG, bool TAIL, int B, int EXTRA = 0, int KSI = 0>
DI void dg_stage(const DGState<C>& st, int t, typename AccT<C::SH>::t (&acc)[C::TN][C::TM], DGFrags<C>& f) {
    dg_step<C, AG, TAIL, B, KSI, EXTRA>(st, t, acc, f);
    if constexpr (KSI + 1 < C::KS) dg_stage<C, AG, TAIL, B, EXTRA, KSI + 1>(st, t, acc, f);
}
template <class C, bool AG, bool TAIL, int B = 0>
DI void dg_group(const DGState<C>& st, int t0, typename AccT<C::SH>::t (&acc)[C::TN][C::TM], DGFrags<C>& f) {
    dg_stage<C, AG, TAIL, B>(st, t0 + B, acc, f);
    if constexpr (B + 1 < C::S) dg_group<C, AG, TAIL, B + 1>(st, t0, acc, f);
}

template <class C>
DI void dg_setup(DGState<C>& st, const char* Xw, int xks, const char* Wg, int ldw, char LDSP* lds) {
    const int tid = threadIdx.x, lane = tid & 63;
    const int wid = __builtin_amdgcn_readfirstlane(tid >> 6);
    st.lds = lds;
    const int q4 = (lane >> 2) & 3;
#pragma unroll
    for (int j = 0; j < C::LW; ++j) {
        const int pc = wid * C::LW + j, kq = pc / (C::BN / 128), a = pc % (C::BN / 128);
        const int cc = C::SH == 32 ? (lane & 3) : ((lane & 3) ^ (2 * ((kq >> 1) & 1)));
        st.woff[j] = (unsigned)(((4 * kq + q4) * ldw + 128 * a + 32 * (lane >> 4) + 8 * cc) * 2);
    }
    st.wbase = Wg;
    st.lds_u = (unsigned)(size_t)lds;
    st.wstride = (long)C::BK * ldw * 2;
    st.wdst = wid * C::LW * 1024;
    st.xbase = Xw;
    if constexpr (C::SH == 32) {
#pragma unroll
        for (int i = 0; i < C::TM32; ++i) st.xoff[i] = lane * 16 + i * xks * 1024;
        const int h = lane >> 5, gg = (lane >> 4) & 1, p = lane & 3, q4r = (lane & 15) >> 2;
        const int n4 = 4 * (p & 1) + 2 * gg + (p >> 1);
        st.a_off[0] = st.a_off[1] = (2 * h * C::NTL) * 256 + q4r * 64 + n4 * 8;
    } else {
        const int g = lane >> 4, p = lane & 3, q4r = (lane & 15) >> 2;
#pragma unroll
        for (int i = 0; i < C::TM32; ++i) st.xoff[i] = (g >> 1) * 1024 + (g & 1) * 512 + (lane & 15) * 16 + i * xks * 1024;
        const int base = (2 * g * C::NTL) * 256 + q4r * 64 + p * 8;
        st.a_off[0] = base + (g & 1) * 32;
        st.a_off[1] = base + ((g & 1) ^ 1) * 32;
    }
}
template <class C>
DI void dg_prologue(const DGState<C>& st, DGFrags<C>& f) {
    static_for<0, C::S - 1>([&](auto ic) {
        constexpr int i = decltype(ic)::value;
        static_for<0, C::LB>([&](auto dc) { dg_bload<C, i, decltype(dc)::value>(st, i, f); });
#pragma unroll
        for (int g = 0; g < C::LW; ++g) dg_glds<C>(st, g, i, i);
    });
}
template <class C, int EXTRA = 0>
DI void dg_start(const DGState<C>& st, DGFrags<C>& f) {
    dg_wait<C, (C::S - 2) * C::LG + EXTRA, 0>(f);
    wg_barrier();
    static_for<0, 2 * C::TN>([&](auto kc) { dg_aread<C>(st, decltype(kc)::value, 0, 0, 0, f); });
    __builtin_amdgcn_sched_barrier(0);
}
template <class C, bool AG>
DI void dg_run(const DGState<C>& st, int nt, typename AccT<C::SH>::t (&acc)[C::TN][C::TM], DGFrags<C>& f) {
    dg_start<C>(st, f);
    int t0 = 0;
    for (; t0 < nt - C::S; t0 += C::S) dg_group<C, AG, false>(st, t0, acc, f);
    dg_group<C, AG, true>(st, t0, acc, f);
    dg_drain_mfma<AG>(acc);
}

using K2Q = DC<256, 128, 8, 4, 32>;
using K2F = DC<256, 128, 8, 4, 16>;
#define K2_ABL 0
#define K2_WARM 7
#define K2_WARM_A ((K2_WARM) & 1)
#define K2_WARM_B ((K2_WARM) & 2)
#define K2_WARM_C ((K2_WARM) & 4)
constexpr int K2_LDS = 160 * 1024;
constexpr int K2_TB = 16384;
DI constexpr int k2_kslot(int j) { return 4 + j % 3; }
DI constexpr int k2_vslot(int j) { return ((j + 1) & 3) == 3 ? 3 : 7 + ((j + 1) & 3); }
constexpr float K2_C = 0.088388347648318440f * 1.4426950408889634f;

struct K2Attn {
    const char* kbase; const char* vbase;
    unsigned koff[2], voff[2];
    unsigned lds_u, dst;
    char LDSP* lds;
    unsigned k_addr, v_addrA, v_addrB;
};
DI void k2_issue_k(const K2Attn& a, int j) {
#pragma unroll
    for (int i = 0; i < 2; ++i) glds16_asm(a.lds_u + k2_kslot(j) * K2_TB + a.dst + i * 1024, a.kbase + (long)j * K2_TB, a.koff[i]);
}
DI void k2_issue_v(const K2Attn& a, int j) {
#pragma unroll
    for (int i = 0; i < 2; ++i) glds16_asm(a.lds_u + k2_vslot(j) * K2_TB + a.dst + i * 1024, a.vbase + (long)j * K2_TB, a.voff[i]);
}
template <int J, int I>
DI bf16x8 k2_kfrag(const K2Attn& a) {
    constexpr int u = I >> 1, kt = I & 1, cu = 4 * (u >> 1) + (u & 1);
    return *(const bf16x8 LDSP*)(a.lds + ((a.k_addr ^ (unsigned)(cu << 4)) + (unsigned)((k2_kslot(J) - 4) * K2_TB + kt * 8192)));
}
template <int J, int I, int HALF>
DI s16x4 k2_vfrag(const K2Attn& a) {
    constexpr int t = I & 3, ks = I >> 2, sl = k2_vslot(J);
    constexpr int off = ((4 * ks + 2 * HALF) * 4 + t) * 256;
    if constexpr (sl == 3) return tr_read(a.lds + off + a.v_addrB);
    else return tr_read(a.lds + ((sl - 6) * K2_TB + off) + a.v_addrA);
}
template <int N> DI void wait_vm() { asm volatile("s_waitcnt vmcnt(%0)" ::"n"(N) : "memory"); }

struct K2Soft {
    f32x16 o[4];
    float m_run, l_run, alpha, mc, ps, tm;
    u32x4 pf[2][4];
    f32x16 T[3];
    bf16x8 kfr[4];
    s16x4 vlo[4], vhi[4];
};
DI float max3_asm(float a, float b, float c) { float r; asm volatile("v_max3_f32 %0, %1, %2, %3" : "=v"(r) : "v"(a), "v"(b), "v"(c)); return r; }
template <class T> DI void anchor(T& x) { asm volatile("" : "+v"(x)); }
template <int PAR, int F, int W>
DI void k2_exp4(K2Soft& z, float s0, float s1, float s2, float s3) {
    const float p0 = __builtin_amdgcn_exp2f(fmaf(s0, K2_C, z.mc)), p1 = __builtin_amdgcn_exp2f(fmaf(s1, K2_C, z.mc));
    const float p2 = __builtin_amdgcn_exp2f(fmaf(s2, K2_C, z.mc)), p3 = __builtin_amdgcn_exp2f(fmaf(s3, K2_C, z.mc));
    z.ps += (p0 + p1) + (p2 + p3);
    unsigned k0 = pk_bf16(p0, p1), k1 = pk_bf16(p2, p3);
    anchor(z.ps); anchor(k0); anchor(k1);
    z.pf[PAR][F][W] = k0;
    z.pf[PAR][F][W + 1] = k1;
}
template <int j, int c>
DI void k2_sm_start(K2Soft& z) {
    f32x16& sa = z.T[(2 * j) % 3]; f32x16& sb = z.T[(2 * j + 1) % 3];
    if constexpr (c < 4) {
        float t = c == 0 ? max3_asm(sa[0], sa[1], sa[2]) : max3_asm(z.tm, sa[4 * c], sa[4 * c + 1]);
        t = c == 0 ? max3_asm(t, sa[3], sa[3]) : max3_asm(t, sa[4 * c + 2], sa[4 * c + 3]);
        t = max3_asm(t, sb[4 * c], sb[4 * c + 1]);
        z.tm = max3_asm(t, sb[4 * c + 2], sb[4 * c + 3]);
    } else if constexpr (c == 4) {
        auto rr = __builtin_amdgcn_permlane32_swap(__float_as_uint(z.tm), __float_as_uint(z.tm), false, false);
        const float m_new = fmaxf(z.m_run, fmaxf(__uint_as_float(rr[0]), __uint_as_float(rr[1])));
        z.alpha = __builtin_amdgcn_exp2f((z.m_run - m_new) * K2_C);
        z.m_run = m_new;
        z.mc = -m_new * K2_C;
        z.ps = 0.f;
        anchor(z.alpha); anchor(z.mc);
    } else if constexpr (c == 5 || c == 7 || c == 9 || c == 11) {
        constexpr int k = (c - 5) / 2;
        k2_exp4<j & 1, (k >> 1), 2 * (k & 1)>(z, sa[4 * k], sa[4 * k + 1], sa[4 * k + 2], sa[4 * k + 3]);
    }
}
template <int j, int c>
DI void k2_sm_finish(K2Soft& z) {
    f32x16& sb = z.T[(2 * j + 1) % 3];
    if constexpr (c == 0 || c == 2 || c == 4 || c == 6) {
        constexpr int k = c / 2;
        k2_exp4<j & 1, 2 + (k >> 1), 2 * (k & 1)>(z, sb[4 * k], sb[4 * k + 1], sb[4 * k + 2], sb[4 * k + 3]);
    } else if constexpr (c == 8) {
        z.l_run = z.l_run * z.alpha + z.ps;
        anchor(z.l_run);
    }
}
template <int j>
DI void k2_phaseA(const K2Attn& a, const bf16x8 (&qf)[8], K2Soft& z) {
    constexpr int PFK = 3;
    f32x16& sa = z.T[(2 * j) % 3]; f32x16& sb = z.T[(2 * j + 1) % 3];
    if constexpr (j < 8) {
        static_for<0, PFK>([&](auto ic) { constexpr int i = decltype(ic)::value; z.kfr[i & 3] = k2_kfrag<(j < 8 ? j : 0), i>(a); });
        __builtin_amdgcn_sched_barrier(0);
    }
    static_for<0, 16>([&](auto ic) {
        constexpr int i = decltype(ic)::value;
        if constexpr (j < 8) {
            if constexpr (i == 0) sa = MFMA32(z.kfr[0], qf[0], (f32x16){});
            else if constexpr (i == 1) sb = MFMA32(z.kfr[1], qf[0], (f32x16){});
            else if constexpr (i & 1) sb = MFMA32(z.kfr[i & 3], qf[i >> 1], sb);
            else sa = MFMA32(z.kfr[i & 3], qf[i >> 1], sa);
#if !(K2_ABL & 2)
            if constexpr (i + PFK < 16) z.kfr[(i + PFK) & 3] = k2_kfrag<(j < 8 ? j : 0), (i + PFK < 16 ? i + PFK : 0)>(a);
#endif
        }
#if !(K2_ABL & 1)
        if constexpr (j >= 1) k2_sm_finish<j - 1, i>(z);
#endif
        if constexpr (j >= 2 && !(K2_ABL & 4)) {
#pragma unroll
            for (int u = 0; u < 4; ++u) { float v = z.o[i >> 2][4 * (i & 3) + u] * z.alpha; anchor(v); z.o[i >> 2][4 * (i & 3) + u] = v; }
        }
        __builtin_amdgcn_sched_barrier(0);
    });
}
template <int j>
DI void k2_phaseB(const K2Attn& a, K2Soft& z) {
    constexpr int PFV = 2;
    if constexpr (j >= 1) {
        static_for<0, PFV>([&](auto ic) { constexpr int i = decltype(ic)::value;
            z.vlo[i & 3] = k2_vfrag<(j >= 1 ? j - 1 : 0), i, 0>(a); z.vhi[i & 3] = k2_vfrag<(j >= 1 ? j - 1 : 0), i, 1>(a); });
    }
    if constexpr (j < 8) asm volatile("s_nop 11");
    __builtin_amdgcn_sched_barrier(0);
    static_for<0, 16>([&](auto ic) {
        constexpr int i = decltype(ic)::value;
        if constexpr (j >= 1) {
            z.o[i & 3] = MFMA32(cat8(z.vlo[i & 3], z.vhi[i & 3]), __builtin_bit_cast(bf16x8, z.pf[(j - 1) & 1][i >> 2]), z.o[i & 3]);
#if !(K2_ABL & 2)
            if constexpr (i + PFV < 16) {
                z.vlo[(i + PFV) & 3] = k2_vfrag<(j >= 1 ? j - 1 : 0), (i + PFV < 16 ? i + PFV : 0), 0>(a);
                z.vhi[(i + PFV) & 3] = k2_vfrag<(j >= 1 ? j - 1 : 0), (i + PFV < 16 ? i + PFV : 0), 1>(a);
            }
#endif
        }
#if !(K2_ABL & 1)
        if constexpr (j < 8) k2_sm_start<(j < 8 ? j : 0), i>(z);
#else
        if constexpr (j < 8 && i == 5) {
#pragma unroll
            for (int f = 0; f < 4; ++f)
#pragma unroll
                for (int w = 0; w < 4; ++w) z.pf[j & 1][f][w] = __float_as_uint(f < 2 ? z.T[(2 * j) % 3][4 * f + w] : z.T[(2 * j + 1) % 3][4 * f + w]);
        }
#endif
        __builtin_amdgcn_sched_barrier(0);
    });
}

__global__ __launch_bounds__(512, 2) void k2_kernel(const u16* __restrict__ xn, const u16* __restrict__ wq, const u16* __restrict__ kc, const u16* __restrict__ vc,
                                                    const u16* __restrict__ w1, u16* __restrict__ o_out, u16* __restrict__ h_out,
                                                    const u16* __restrict__ wo, const u16* __restrict__ w2
                                                    ) {
    __shared__ __attribute__((aligned(1024))) char smem[K2_LDS];
    const int bid = blockIdx.x;
    const int head = (bid & 7) * 4 + ((bid >> 3) & 3), qb = bid >> 5;
    const int wid = __builtin_amdgcn_readfirstlane(threadIdx.x >> 6);
    char LDSP* lds = (char LDSP*)smem;
    const char* Xw = (const char*)xn + (size_t)(qb * 8 + wid) * 64 * 1024;
    f32x16 accq[4][1];
    dg_zero_acc<false>(accq);
    K2Attn a;
    {
        DGState<K2Q> st; DGFrags<K2Q> f;
        dg_setup<K2Q>(st, Xw, 64, (const char*)(wq + head * 128), 4096, lds);
        dg_prologue<K2Q>(st, f);
        {
#pragma unroll
            for (int u = 0; u < 2; ++u) {
                const int sg = 3 + 2 * qb + u < 15 ? 3 + 2 * qb + u : 15;
#pragma unroll
                for (int g = 0; g < 2; ++g) glds16_asm(st.lds_u + 7 * K2_TB + st.wdst + g * 1024, st.wbase + (long)sg * st.wstride, st.woff[g]);
            }
        }
        {
            const int lane = threadIdx.x & 63, kvh = head >> 3;
            a.kbase = (const char*)(kc + ((size_t)kvh * 2048 + 1536) * 128);
            a.vbase = (const char*)(vc + ((size_t)kvh * 2048 + 1536) * 128);
            a.lds = lds; a.lds_u = (unsigned)(size_t)lds; a.dst = wid * 2048;
#pragma unroll
            for (int i = 0; i < 2; ++i) {
                const int pc = wid * 2 + i;
                const int key = 4 * pc + (lane >> 4), c = (lane & 15) ^ (key & 15);
                a.koff[i] = key * 256 + c * 16;
                const int kv = 4 * pc + ((lane >> 2) & 3);
                a.voff[i] = kv * 256 + ((lane >> 4) * 32 + (lane & 3) * 8) * 2;
            }
        }
        dg_start<K2Q, K2_WARM_A ? 4 : 0>(st, f);
        static_for<0, 16>([&](auto tc) {
            constexpr int t = decltype(tc)::value;
            constexpr auto hook = [](int u) { return (u >= 1 && u <= 5) ? 2 : (u == -1 && K2_WARM_A ? 4 : 0); };
            dg_stage<K2Q, false, (t >= 12), (t & 3), hook(t - 1) + hook(t - 2)>(st, t, accq, f);
            if constexpr (t == 1) k2_issue_k(a, 0);
            if constexpr (t == 2) k2_issue_v(a, 0);
            if constexpr (t == 3) k2_issue_k(a, 1);
            if constexpr (t == 4) k2_issue_v(a, 1);
            if constexpr (t == 5) k2_issue_k(a, 2);
        });
        dg_drain_mfma<false>(accq);
    }
    bf16x8 qf[8];
#pragma unroll
    for (int u = 0; u < 8; ++u) {
        u32x4 w;
#pragma unroll
        for (int i = 0; i < 4; ++i) w[i] = pk_bf16(accq[u >> 1][0][8 * (u & 1) + 2 * i], accq[u >> 1][0][8 * (u & 1) + 2 * i + 1]);
        qf[u] = __builtin_bit_cast(bf16x8, w);
    }
    {
        int tz = threadIdx.x; asm volatile("" : "+v"(tz));
        const int lane = tz & 63, h = lane >> 5;
        a.k_addr = 4 * K2_TB + (lane & 31) * 256 + ((((2 * h) ^ (lane & 15)) & 15) << 4);
        const int gg = (lane >> 4) & 1, p = lane & 3, q4 = (lane & 15) >> 2;
        const int n4 = 4 * (p & 1) + 2 * gg + (p >> 1);
        const unsigned vl = h * 4 * 256 + q4 * 64 + n4 * 8;
        a.v_addrA = 6 * K2_TB + vl; a.v_addrB = 3 * K2_TB + vl;
        asm volatile("" : "+v"(a.k_addr), "+v"(a.v_addrA), "+v"(a.v_addrB));
    }
    K2Soft z;
#pragma unroll
    for (int t = 0; t < 4; ++t) z.o[t] = (f32x16){};
    z.m_run = -INFINITY; z.l_run = 0.f; z.alpha = 1.f; z.mc = 0.f; z.ps = 0.f;
    const bool hasF = head < 28;
    f32x4 accf[8][2];
    DGState<K2F> stf; DGFrags<K2F> ff;
    auto top = [&](auto ic) {
        constexpr int i = decltype(ic)::value;
        if constexpr (i >= 3) wait_vm<(i - 1 <= 5 ? 4 : (i - 1 == 6 ? 2 : 0))>();
        wg_barrier();
        if constexpr (i == 0) {
            if (hasF) {
                DGState<K2F> sw;
                dg_setup<K2F>(sw, Xw, 64, (const char*)(w1 + head * 128), 3584, lds);
#pragma unroll
                for (int u = 0; u < 2; ++u)
#pragma unroll
                    for (int g = 0; g < 2; ++g) glds16_asm(sw.lds_u + sw.wdst + g * 1024, sw.wbase + (long)(2 * qb + u) * sw.wstride, sw.woff[g]);
            }
        }
        if constexpr (i >= 1 && i <= 5) k2_issue_k(a, i + 2);
        if constexpr (i >= 1 && i <= 6) k2_issue_v(a, i + 1);
        if constexpr (i < 8) {
            int pc = (bid * 8 + wid) * 8 + i;
            pc = pc < 15360 ? pc : 15359;
            const char* src = pc < 8192 ? (const char*)wo + (size_t)pc * 1024 : (const char*)w2 + (size_t)(pc - 8192) * 1024;
            int tz = threadIdx.x; asm volatile("" : "+v"(tz));
            glds16_asm(a.lds_u + K2_TB + wid * 1024, src, (unsigned)(tz & 63) * 16);
        }
        if constexpr (i == 8) {
            if (hasF) {
                dg_setup<K2F>(stf, Xw, 64, (const char*)(w1 + head * 128), 3584, lds);
                dg_prologue<K2F>(stf, ff);
            }
        }
    };
#define K2_STAMP(n)
    if (wid < 4) {
        top(std::integral_constant<int, 0>{});
        static_for<0, 9>([&](auto jc) {
            constexpr int j = decltype(jc)::value;
            K2_STAMP(4 * j);
            if constexpr (j >= 1) top(jc);
            K2_STAMP(4 * j + 1);
            k2_phaseA<j>(a, qf, z);
            K2_STAMP(4 * j + 2);
            k2_phaseB<j>(a, z);
            K2_STAMP(4 * j + 3);
        });
    } else {
        top(std::integral_constant<int, 0>{});
        static_for<0, 9>([&](auto jc) {
            constexpr int j = decltype(jc)::value;
            K2_STAMP(4 * j + 1);
            k2_phaseA<j>(a, qf, z);
            K2_STAMP(4 * j + 2);
            if constexpr (j < 8) top(std::integral_constant<int, j + 1>{});
            K2_STAMP(4 * j);
            k2_phaseB<j>(a, z);
            K2_STAMP(4 * j + 3);
        });
    }
    {
        int tz = threadIdx.x; asm volatile("" : "+v"(tz));
        const int lane = tz & 63;
        auto rr = __builtin_amdgcn_permlane32_swap(__float_as_uint(z.l_run), __float_as_uint(z.l_run), false, false);
        const float inv = 1.0f / (__uint_as_float(rr[0]) + __uint_as_float(rr[1]));
        const __amdgpu_buffer_rsrc_t ors = __builtin_amdgcn_make_buffer_rsrc(o_out, 0, 2048u * 4096u * 2u, 0x00020000);
        const unsigned obase = (unsigned)(((qb * 8 + wid) * 256 + head * 8) * 1024);
#pragma unroll
        for (int t = 0; t < 4; ++t) {
#pragma unroll
            for (int r = 0; r < 16; ++r) z.o[t][r] *= inv;
            store_tile_frag_wt(ors, obase + t * 2048, z.o[t], lane);
        }
    }
    if (hasF) {
        dg_zero_acc<false>(accf);
        dg_start<K2F>(stf, ff);
        dg_group<K2F, false, false>(stf, 0, accf, ff);
        dg_group<K2F, false, false>(stf, 4, accf, ff);
        dg_group<K2F, false, false>(stf, 8, accf, ff);
        dg_group<K2F, false, true>(stf, 12, accf, ff);
        dg_drain_mfma<false>(accf);
        int tz = threadIdx.x; asm volatile("" : "+v"(tz));
        const int lane = tz & 63;
        char* base = (char*)h_out + ((size_t)(qb * 8 + wid) * 224 + head * 8) * 1024;
#pragma unroll
        for (int x = 0; x < 8; ++x)
#pragma unroll
            for (int y = 0; y < 2; ++y) store_tile_frag<true>(base + x * 1024, accf[x][y], lane, y);
    }
}

using K3C = DC<128, 128, 4, 4, 16>;
constexpr int K3_STG_PITCH = 272;
constexpr int K3_STG_WAVE = 32 * K3_STG_PITCH;
constexpr int K3_LDS = K3C::LDS_BYTES + 4 * K3_STG_WAVE + 16;

typedef __attribute__((address_space(1))) unsigned gu32;
DI unsigned atomic_load_agent(unsigned* p) { return __hip_atomic_load(p, __ATOMIC_RELAXED, __HIP_MEMORY_SCOPE_AGENT); }
DI unsigned atomic_add_agent(unsigned* p, unsigned v) { return __hip_atomic_fetch_add(p, v, __ATOMIC_RELAXED, __HIP_MEMORY_SCOPE_AGENT); }

DI float silu_bf(float a) {
    const float x = rbf(a);
    const float e = __builtin_amdgcn_exp2f(-x * 1.4426950408889634f);
    return rbf(x * __builtin_amdgcn_rcpf(1.0f + e));
}

DI bool k3_exchange(const u32x4 (&mine)[8], u32x4 (&theirs)[8], unsigned* cnt, char* my_slot, char* their_slot, int lane, int wid, volatile unsigned LDSP* word) {
    if (wid == 0) {
        const unsigned c = atomic_load_agent(cnt);
        if (lane == 0) *word = c;
    }
    __syncthreads();
    unsigned c = __builtin_amdgcn_readfirstlane(*word);
    if (c == 0) {
        const __amdgpu_buffer_rsrc_t rs = __builtin_amdgcn_make_buffer_rsrc(my_slot, 0, 8192u, 0x00020000);
#pragma unroll
        for (int i = 0; i < 8; ++i) __builtin_amdgcn_raw_buffer_store_b128(mine[i], rs, i * 1024 + lane * 16, 0, 16  );
        asm volatile("s_waitcnt vmcnt(0)" ::: "memory");
        __syncthreads();
        if (wid == 0) {
            unsigned old = 0;
            if (lane == 0) old = atomic_add_agent(cnt, 1u);
            if (lane == 0) *word = old;
        }
        __syncthreads();
        c = __builtin_amdgcn_readfirstlane(*word);
        if (c == 0) return false;
    }
    const __amdgpu_buffer_rsrc_t rt = __builtin_amdgcn_make_buffer_rsrc(their_slot, 0, 8192u, 0x00020000);
#pragma unroll
    for (int i = 0; i < 8; ++i) theirs[i] = __builtin_amdgcn_raw_buffer_load_b128(rt, i * 1024 + lane * 16, 0, 16  );
    return true;
}

__global__ __launch_bounds__(256, 1) void k3_kernel(const u16* __restrict__ o_f, const u16* __restrict__ wo, const u16* __restrict__ h_f,
                                                    const u16* __restrict__ w2, const u16* __restrict__ x, u16* __restrict__ out,
                                                    char* __restrict__ exs, char* __restrict__ exg, unsigned* __restrict__ cnts
                                                    ) {
    using C = K3C;
    __shared__ __attribute__((aligned(1024))) char smem[K3_LDS];
    const int bid = blockIdx.x, xcd = bid & 7, i5 = bid >> 3;
    const int type = xcd >> 2, pm = (xcd & 3) * 4 + (i5 & 3), pn = i5 >> 2;
    const int tile = pm * 8 + pn;
    const int wid = __builtin_amdgcn_readfirstlane(threadIdx.x >> 6);
    f32x4 acc[C::TN][C::TM];
    dg_zero_acc<true>(acc);
    const int Kd = type == 0 ? 4096 : 3584;
    {
        const char* Xw = (const char*)(type == 0 ? o_f : h_f) + (size_t)(pm * 4 + wid) * (Kd / 16) * 1024;
        const u16* W = (type == 0 ? wo : w2) + pn * 128;
        DGState<C> st; DGFrags<C> f;
        dg_setup<C>(st, Xw, Kd / 16, (const char*)W, 1024, (char LDSP*)smem);
        dg_prologue<C>(st, f);
        dg_run<C, true>(st, Kd / 64, acc, f);
    }
    int tz = threadIdx.x; asm volatile("" : "+v"(tz));
    const int lane = tz & 63;
    const size_t row0 = (size_t)pm * 128 + wid * 32;
    const u16* xp = x + (row0 + (lane >> 4)) * 1024 + pn * 128 + (lane & 15) * 8;
    u32x4 xv[8];
#pragma unroll
    for (int i = 0; i < 8; ++i) xv[i] = *(const u32x4*)(xp + (size_t)i * 4 * 1024);
    u32x4 mine[8], theirs[8];
#pragma unroll
    for (int i = 0; i < 8; ++i) {
        if (type == 0) mine[i] = (u32x4){pk_bf16(acc[i][0][0], acc[i][0][1]), pk_bf16(acc[i][0][2], acc[i][0][3]), pk_bf16(acc[i][1][0], acc[i][1][1]), pk_bf16(acc[i][1][2], acc[i][1][3])};
        else mine[i] = (u32x4){pk_bf16(silu_bf(acc[i][0][0]), silu_bf(acc[i][0][1])), pk_bf16(silu_bf(acc[i][0][2]), silu_bf(acc[i][0][3])),
                               pk_bf16(silu_bf(acc[i][1][0]), silu_bf(acc[i][1][1])), pk_bf16(silu_bf(acc[i][1][2]), silu_bf(acc[i][1][3]))};
    }
    const size_t slot = ((size_t)tile * 4 + wid) * 8192;
    if (!k3_exchange(mine, theirs, cnts + tile * 4, (type == 0 ? exs : exg) + slot, (type == 0 ? exg : exs) + slot, lane, wid,
                     (volatile unsigned LDSP*)((char LDSP*)smem + K3_LDS - 16))) {
        return;
    }
    char LDSP* stg = (char LDSP*)smem + C::LDS_BYTES + wid * K3_STG_WAVE;
#pragma unroll
    for (int i = 0; i < 8; ++i)
#pragma unroll
        for (int tb = 0; tb < 2; ++tb) {
            const unsigned a0 = mine[i][2 * tb], a1 = mine[i][2 * tb + 1], b0 = theirs[i][2 * tb], b1 = theirs[i][2 * tb + 1];
            const float p0 = __uint_as_float(a0 << 16) * __uint_as_float(b0 << 16), p1 = __uint_as_float(a0 & 0xffff0000u) * __uint_as_float(b0 & 0xffff0000u);
            const float p2 = __uint_as_float(a1 << 16) * __uint_as_float(b1 << 16), p3 = __uint_as_float(a1 & 0xffff0000u) * __uint_as_float(b1 & 0xffff0000u);
            *(u32x2 LDSP*)(stg + (16 * tb + (lane & 15)) * K3_STG_PITCH + (16 * i + 4 * (lane >> 4)) * 2) = (u32x2){pk_bf16(p0, p1), pk_bf16(p2, p3)};
        }
    u16* op = out + (row0 + (lane >> 4)) * 1024 + pn * 128 + (lane & 15) * 8;
#pragma unroll
    for (int i = 0; i < 8; ++i) {
        const u32x4 pv = *(const u32x4 LDSP*)(stg + (4 * i + (lane >> 4)) * K3_STG_PITCH + (lane & 15) * 16);
        u32x4 ov;
#pragma unroll
        for (int e = 0; e < 4; ++e)
            ov[e] = pk_bf16(__uint_as_float(xv[i][e] << 16) + __uint_as_float(pv[e] << 16), __uint_as_float(xv[i][e] & 0xffff0000u) + __uint_as_float(pv[e] & 0xffff0000u));
        *(u32x4*)(op + (size_t)i * 4 * 1024) = ov;
    }
}

constexpr int M_ = 2048, D_ = 1024;

__global__ __launch_bounds__(256) void ln_kernel(const u16* __restrict__ x, u16* __restrict__ xn, unsigned* __restrict__ cnts) {
    const int lane = threadIdx.x & 63, wid = threadIdx.x >> 6;
    if (blockIdx.x == 0 && threadIdx.x < 128) cnts[threadIdx.x * 4] = 0u;
    const int row = blockIdx.x * 4 + wid;
    const u32x4* src = (const u32x4*)(x + (size_t)row * D_ + lane * 16);
    u32x4 a = src[0], b = src[1];
    float v[16];
#pragma unroll
    for (int i = 0; i < 4; ++i) { v[2 * i] = __uint_as_float(a[i] << 16); v[2 * i + 1] = __uint_as_float(a[i] & 0xffff0000u);
                                  v[8 + 2 * i] = __uint_as_float(b[i] << 16); v[8 + 2 * i + 1] = __uint_as_float(b[i] & 0xffff0000u); }
    float s = 0.f;
#pragma unroll
    for (int i = 0; i < 16; ++i) s += v[i];
#pragma unroll
    for (int o = 32; o >= 1; o >>= 1) s += __shfl_xor(s, o);
    const float mean = s * (1.0f / 1024.0f);
    float q = 0.f;
#pragma unroll
    for (int i = 0; i < 16; ++i) { const float d = v[i] - mean; q += d * d; }
#pragma unroll
    for (int o = 32; o >= 1; o >>= 1) q += __shfl_xor(q, o);
    const float var = q * (1.0f / 1024.0f);
    const float mb = rbf(mean), vb = rbf(var);
    const float den = rbf(sqrtf(rbf(vb + 1e-5f)));
    unsigned o[8];
#pragma unroll
    for (int i = 0; i < 8; ++i) {
        const float d0 = rbf(v[2 * i] - mb), d1 = rbf(v[2 * i + 1] - mb);
        o[i] = pk_bf16(d0 / den, d1 / den);
    }
    char* dst = (char*)xn + ((size_t)(row >> 5) * (D_ / 16) + lane) * 1024 + (row & 31) * 16;
    *(u32x4*)dst = (u32x4){o[0], o[1], o[2], o[3]};
    *(u32x4*)(dst + 512) = (u32x4){o[4], o[5], o[6], o[7]};
}

constexpr size_t WS_XN = 0, WS_H = 4u << 20, WS_O = 20u << 20, WS_EXS = 36u << 20, WS_EXG = 40u << 20, WS_CNT = 44u << 20;

extern "C" void kernel_launch(void* const* d_in, const int* in_sizes, int n_in, void* d_out, int out_size, void* d_ws, size_t ws_size,
                              hipStream_t stream) {
    const u16* x = (const u16*)d_in[0];
    const u16* kc = (const u16*)d_in[1];
    const u16* vc = (const u16*)d_in[2];
    const u16* wq = (const u16*)d_in[3];
    const u16* wo = (const u16*)d_in[4];
    const u16* w1 = (const u16*)d_in[6];
    const u16* w2 = (const u16*)d_in[7];
    char* ws = (char*)d_ws;
    u16* xn = (u16*)(ws + WS_XN); u16* h = (u16*)(ws + WS_H); u16* o = (u16*)(ws + WS_O);
    unsigned* cnts = (unsigned*)(ws + WS_CNT);
    ln_kernel<<<512, 256, 0, stream>>>(x, xn, cnts);
    k2_kernel<<<256, 512, 0, stream>>>(xn, wq, kc, vc, w1, o, h, wo, w2);
    k3_kernel<<<256, 256, 0, stream>>>(o, wo, h, w2, x, (u16*)d_out, ws + WS_EXS, ws + WS_EXG, cnts);
}
extern "C" void launch_one(int which, void* const* d_in, void* d_out, void* d_ws, hipStream_t stream) {
    const u16* x = (const u16*)d_in[0];
    const u16* kc = (const u16*)d_in[1];
    const u16* vc = (const u16*)d_in[2];
    const u16* wq = (const u16*)d_in[3];
    const u16* wo = (const u16*)d_in[4];
    const u16* w1 = (const u16*)d_in[6];
    const u16* w2 = (const u16*)d_in[7];
    char* ws = (char*)d_ws;
    u16* xn = (u16*)(ws + WS_XN); u16* h = (u16*)(ws + WS_H); u16* o = (u16*)(ws + WS_O);
    unsigned* cnts = (unsigned*)(ws + WS_CNT);
    if (which == 0) ln_kernel<<<512, 256, 0, stream>>>(x, xn, cnts);
    if (which == 1) k2_kernel<<<256, 512, 0, stream>>>(xn, wq, kc, vc, w1, o, h, wo, w2);
    if (which == 2) { ln_kernel<<<1, 256, 0, stream>>>(x, xn, cnts); k3_kernel<<<256, 256, 0, stream>>>(o, wo, h, w2, x, (u16*)d_out, ws + WS_EXS, ws + WS_EXG, cnts); }
}
```

```cpp
#include <hip/hip_runtime.h>
#include <stdint.h>

typedef short bf16x8 __attribute__((ext_vector_type(8)));
typedef short s16x4 __attribute__((ext_vector_type(4)));
typedef float f32x16 __attribute__((ext_vector_type(16)));
typedef float f32x2 __attribute__((ext_vector_type(2)));
typedef unsigned u32x4 __attribute__((ext_vector_type(4)));
typedef unsigned short u16;
typedef __bf16 bf16x2_t __attribute__((ext_vector_type(2)));
#define LDSP __attribute__((address_space(3)))
#define DI __device__ __forceinline__

DI float bf2f(u16 v) { return __uint_as_float(((unsigned)v) << 16); }
DI unsigned pk_bf16(float lo, float hi) {
    f32x2 f = {lo, hi};
    bf16x2_t b = __builtin_convertvector(f, bf16x2_t);
    return __builtin_bit_cast(unsigned, b);
}
DI float rbf(float x) {
    unsigned p = pk_bf16(x, x);
    return __uint_as_float(p << 16);
}
DI s16x4 tr_read(const char LDSP* p) { return __builtin_amdgcn_ds_read_tr16_b64_v4i16((s16x4 LDSP*)p); }
DI bf16x8 cat8(s16x4 lo, s16x4 hi) { return __builtin_shufflevector(lo, hi, 0, 1, 2, 3, 4, 5, 6, 7); }
#define MFMA32(a, b, c) __builtin_amdgcn_mfma_f32_32x32x16_bf16((a), (b), (c), 0, 0, 0)

#define TL_START(k)
#define TL_END(k)

#include <type_traits>
template <int I, int N, class F>
DI void static_for(F&& f) {
    if constexpr (I < N) {
        f(std::integral_constant<int, I>{});
        static_for<I + 1, N>(f);
    }
}
typedef float f32x4 __attribute__((ext_vector_type(4)));
#define MFMA16(a, b, c) __builtin_amdgcn_mfma_f32_16x16x32_bf16((a), (b), (c), 0, 0, 0)

template <int BM_, int BN_, int BK_, int NWM_, int NWN_, int S_, int SH_, int ABL_ = 0>
struct GC {
    static constexpr int ABL = ABL_;
    static constexpr int BM = BM_, BN = BN_, BK = BK_, NWM = NWM_, NWN = NWN_, S = S_, SH = SH_;
    static constexpr int NW = NWM * NWN, NT = NW * 64;
    static constexpr int TT = SH, KK = SH == 32 ? 16 : 32;
    static constexpr int TM = BM / TT / NWM, TN = BN / TT / NWN, KS = BK / KK, NTL = BN / 32, KS16 = BK / 16;
    static constexpr int XB = BM * BK * 2, WB = BN * BK * 2, SB = XB + WB;
    static constexpr int XP = XB / 1024, WP = WB / 1024, XPW = XP / NW, WPW = WP / NW, L = XPW + WPW;
    static constexpr int LDS_BYTES = S * SB;
    static constexpr int NM = TN * TM, NR = 2 * TN + TM;
    static_assert(XP % NW == 0 && WP % NW == 0, "pieces per wave");
    static_assert((S * KS) % 2 == 0, "fragment double buffer parity");
    static_assert(BN % 128 == 0, "BN");
    static_assert(SH == 32 || (TM % 2 == 0 && TN % 2 == 0), "TM/TN even for the 16x16 shape");
};
template <int SH> struct AccT;
template <> struct AccT<32> { typedef f32x16 t; };
template <> struct AccT<16> { typedef f32x4 t; };

template <class C>
struct GemmState {
    const char* xsrc[C::XPW];
    const char* wsrc[C::WPW];
    long xstride, wstride;
    char LDSP* lds;
    unsigned xdst, wdst;
    unsigned a_off[2], b_off;
};

template <int N> DI void wait_vm_lgkm0() { asm volatile("s_waitcnt vmcnt(%0) lgkmcnt(0)" ::"n"(N) : "memory"); }
DI void wg_barrier() {
    asm volatile("" ::: "memory");
    __builtin_amdgcn_s_barrier();
    asm volatile("" ::: "memory");
}

template <class C>
struct GemmFrags {
    s16x4 alo[2][C::TN], ahi[2][C::TN];
    bf16x8 b[2][C::TM];
};
template <class C>
DI void gemm_read_op(const GemmState<C>& st, int k, int buf, int ks, int sel, GemmFrags<C>& f) {
    if (k >= 2 && k < 2 + C::TM) {
        const int tb = k - 2;
        if constexpr (C::SH == 32)
            f.b[sel][tb] = *(const bf16x8 LDSP*)(st.lds + (buf * C::SB + (tb * C::KS16 + ks) * 1024) + st.b_off);
        else
            f.b[sel][tb] = *(const bf16x8 LDSP*)(st.lds + (buf * C::SB + ((tb >> 1) * C::KS16 + 2 * ks) * 1024 + (tb & 1) * 256) + st.b_off);
    } else {
        const int idx = k < 2 ? k : k - C::TM, ta = idx >> 1, half = idx & 1;
        const char LDSP* p;
        if constexpr (C::SH == 32) p = st.lds + (buf * C::SB + C::XB + ((4 * ks + half) * C::NTL + ta) * 256) + st.a_off[0];
        else p = st.lds + (buf * C::SB + C::XB + ((8 * ks + half) * C::NTL + (ta >> 1)) * 256) + st.a_off[ta & 1];
        if (half) f.ahi[sel][ta] = tr_read(p); else f.alo[sel][ta] = tr_read(p);
    }
}
template <class C>
DI void gemm_glds_op(const GemmState<C>& st, int g, int t, int buf) {
    if (g < C::XPW)
        __builtin_amdgcn_global_load_lds((const unsigned*)(st.xsrc[g] + (long)t * st.xstride),
                                         (unsigned LDSP*)(st.lds + buf * C::SB + st.xdst + g * 1024), 16, 0, 0);
    else
        __builtin_amdgcn_global_load_lds((const unsigned*)(st.wsrc[g - C::XPW] + (long)t * st.wstride),
                                         (unsigned LDSP*)(st.lds + buf * C::SB + C::XB + st.wdst + (g - C::XPW) * 1024), 16, 0, 0);
}
template <class C> constexpr int read_slot(int k) {
    constexpr int SPAN = C::NM - (C::NM / 4 > 1 ? C::NM / 4 : 1);
    return k * SPAN / C::NR;
}
template <class C> constexpr int glds_slot(int g) {
    constexpr int OFF = C::NM / 4;
    return OFF + g * (C::NM - OFF) / C::L;
}

template <class C, int B, int KSI, bool TAIL>
DI void gemm_step(const GemmState<C>& st, int t, typename AccT<C::SH>::t (&acc)[C::TN][C::TM], GemmFrags<C>& f) {
    constexpr int cur = (C::ABL & 1) ? 0 : ((B * C::KS + KSI) & 1), nxt = cur ^ 1;
    constexpr bool LAST = KSI == C::KS - 1;
    constexpr bool HAS_NEXT = !(LAST && TAIL && B == C::S - 1);
    constexpr bool SYNC = LAST && HAS_NEXT, ISSUE = SYNC && !TAIL;
    constexpr int nbuf = LAST ? (B + 1) % C::S : B, nks = LAST ? 0 : KSI + 1;
    if constexpr (SYNC) {
        constexpr int inflight = TAIL ? (C::S - B - 2) : (C::S - 2);
        wait_vm_lgkm0<inflight * C::L>();
        wg_barrier();
    }
    static_for<0, C::NM>([&](auto ic) {
        constexpr int i = decltype(ic)::value, ta = i / C::TM, tb = i % C::TM;
        if constexpr (C::ABL & 4) { asm volatile("" :: "v"(f.alo[cur][ta]), "v"(f.ahi[cur][ta]), "v"(f.b[cur][tb])); }
        else if constexpr (C::SH == 32) acc[ta][tb] = MFMA32(cat8(f.alo[cur][ta], f.ahi[cur][ta]), f.b[cur][tb], acc[ta][tb]);
        else acc[ta][tb] = MFMA16(cat8(f.alo[cur][ta], f.ahi[cur][ta]), f.b[cur][tb], acc[ta][tb]);
        if constexpr (HAS_NEXT && !(C::ABL & 1)) {
            static_for<0, C::NR>([&](auto kc) {
                constexpr int k = decltype(kc)::value;
                if constexpr (read_slot<C>(k) == i) gemm_read_op<C>(st, k, nbuf, nks, nxt, f);
            });
        }
        if constexpr (ISSUE && !(C::ABL & 2)) {
            static_for<0, C::L>([&](auto gc) {
                constexpr int g = decltype(gc)::value;
                if constexpr (glds_slot<C>(g) == i) gemm_glds_op<C>(st, g, t + C::S, B);
            });
        }
        __builtin_amdgcn_sched_barrier(0);
    });
}

template <class C, int B, bool TAIL, int KSI = 0>
DI void gemm_stage_body(const GemmState<C>& st, int t, typename AccT<C::SH>::t (&acc)[C::TN][C::TM], GemmFrags<C>& f) {
    gemm_step<C, B, KSI, TAIL>(st, t, acc, f);
    if constexpr (KSI + 1 < C::KS) gemm_stage_body<C, B, TAIL, KSI + 1>(st, t, acc, f);
}
template <class C, bool TAIL, int B = 0>
DI void gemm_group(const GemmState<C>& st, int t0, typename AccT<C::SH>::t (&acc)[C::TN][C::TM], GemmFrags<C>& f) {
    gemm_stage_body<C, B, TAIL>(st, t0 + B, acc, f);
    if constexpr (B + 1 < C::S) gemm_group<C, TAIL, B + 1>(st, t0, acc, f);
}
template <class C, int B = 0>
DI void gemm_prologue_issue(const GemmState<C>& st) {
#pragma unroll
    for (int g = 0; g < C::L; ++g) gemm_glds_op<C>(st, g, B, B);
    if constexpr (B + 1 < C::S) gemm_prologue_issue<C, B + 1>(st);
}

template <class C>
DI void gemm_core(const char* __restrict__ Xg, int xks, const char* __restrict__ Wg, int ldw, int nt, char* smem,
                  typename AccT<C::SH>::t (&acc)[C::TN][C::TM], unsigned long long& T1) {
    const int tid = threadIdx.x, lane = tid & 63;
    const int wid = __builtin_amdgcn_readfirstlane(tid >> 6);
    const int wm = wid / C::NWN, wn = wid % C::NWN;
    GemmState<C> st;
    st.lds = (char LDSP*)smem;
#pragma unroll
    for (int i = 0; i < C::XPW; ++i) {
        const int pc = wid * C::XPW + i, mt = pc / C::KS16, ks = pc % C::KS16;
        st.xsrc[i] = Xg + ((long)mt * xks + ks) * 1024 + lane * 16;
    }
    {
        const int q4 = (lane >> 2) & 3;
#pragma unroll
        for (int j = 0; j < C::WPW; ++j) {
            const int pc = wid * C::WPW + j, kq = pc / (C::BN / 128), a = pc % (C::BN / 128);
            const int cc = C::SH == 32 ? (lane & 3) : ((lane & 3) ^ (2 * ((kq >> 1) & 1)));
            st.wsrc[j] = Wg + ((long)(4 * kq + q4) * ldw + 128 * a + 32 * (lane >> 4) + 8 * cc) * 2;
        }
    }
    st.xstride = (long)C::KS16 * 1024;
    st.wstride = (long)C::BK * ldw * 2;
    st.xdst = wid * C::XPW * 1024;
    st.wdst = wid * C::WPW * 1024;
    if constexpr (C::SH == 32) {
        const int h = lane >> 5, gg = (lane >> 4) & 1, p = lane & 3, q4 = (lane & 15) >> 2;
        const int n4 = 4 * (p & 1) + 2 * gg + (p >> 1);
        st.a_off[0] = (2 * h * C::NTL) * 256 + q4 * 64 + n4 * 8 + wn * C::TN * 256;
        st.a_off[1] = st.a_off[0];
        st.b_off = lane * 16 + wm * C::TM * C::KS16 * 1024;
    } else {
        const int g = lane >> 4, p = lane & 3, q4 = (lane & 15) >> 2;
        const int base = (2 * g * C::NTL) * 256 + q4 * 64 + p * 8 + wn * (C::TN / 2) * 256;
        st.a_off[0] = base + (g & 1) * 32;
        st.a_off[1] = base + ((g & 1) ^ 1) * 32;
        st.b_off = (g >> 1) * 1024 + (g & 1) * 512 + (lane & 15) * 16 + wm * (C::TM / 2) * C::KS16 * 1024;
    }
    GemmFrags<C> f;
    gemm_prologue_issue<C>(st);
    wait_vm_lgkm0<(C::S - 1) * C::L>();
    wg_barrier();
    T1 = __builtin_amdgcn_s_memtime();
#pragma unroll
    for (int k = 0; k < C::NR; ++k) gemm_read_op<C>(st, k, 0, 0, 0, f);
    __builtin_amdgcn_sched_barrier(0);
    int t0 = 0;
    for (; t0 < nt - C::S; t0 += C::S) gemm_group<C, false>(st, t0, acc, f);
    gemm_group<C, true>(st, t0, acc, f);
}

template <bool RELU>
DI void store_tile_frag(char* base, const f32x16& a, int lane, int  ) {
    const int h = lane >> 5;
    char* p = base + h * 1024 + (lane & 31) * 16;
    unsigned w[8];
#pragma unroll
    for (int i = 0; i < 8; ++i) {
        float lo = a[2 * i], hi = a[2 * i + 1];
        if (RELU) { lo = fmaxf(lo, 0.f); hi = fmaxf(hi, 0.f); }
        w[i] = pk_bf16(lo, hi);
    }
    *(u32x4*)p = (u32x4){w[0], w[1], w[2], w[3]};
    *(u32x4*)(p + 512) = (u32x4){w[4], w[5], w[6], w[7]};
}
DI void store_tile_frag_wt(__amdgpu_buffer_rsrc_t rs, unsigned base_off, const f32x16& a, int lane) {
    const unsigned p = base_off + (lane >> 5) * 1024 + (lane & 31) * 16;
    u32x4 w0, w1;
#pragma unroll
    for (int i = 0; i < 4; ++i) { w0[i] = pk_bf16(a[2 * i], a[2 * i + 1]); w1[i] = pk_bf16(a[8 + 2 * i], a[8 + 2 * i + 1]); }
    __builtin_amdgcn_raw_buffer_store_b128(w0, rs, p, 0, 16  );
    __builtin_amdgcn_raw_buffer_store_b128(w1, rs, p + 512, 0, 16  );
}
typedef unsigned u32x2 __attribute__((ext_vector_type(2)));
template <bool RELU>
DI void store_tile_frag(char* base, const f32x4& a, int lane, int mhalf) {
    const int g = lane >> 4;
    char* p = base + (g >> 1) * 512 + (16 * mhalf + (lane & 15)) * 16 + (g & 1) * 8;
    float v0 = a[0], v1 = a[1], v2 = a[2], v3 = a[3];
    if (RELU) { v0 = fmaxf(v0, 0.f); v1 = fmaxf(v1, 0.f); v2 = fmaxf(v2, 0.f); v3 = fmaxf(v3, 0.f); }
    *(u32x2*)p = (u32x2){pk_bf16(v0, v1), pk_bf16(v2, v3)};
}

template <int BM_, int BN_, int NW_, int S_, int SH_>
struct DC {
    static constexpr int BM = BM_, BN = BN_, NW = NW_, S = S_, SH = SH_, BK = 64;
    static constexpr int NT = NW * 64;
    static constexpr int TT = SH, KK = SH == 32 ? 16 : 32;
    static constexpr int TW = BM / NW, TM = TW / TT, TN = BN / TT, KS = BK / KK, NTL = BN / 32;
    static constexpr int TM32 = TW / 32;
    static constexpr int WB = BN * BK * 2, WP = WB / 1024, LW = WP / NW;
    static constexpr int LB = TM * KS;
    static constexpr int LG = LW + LB;
    static constexpr int LDS_BYTES = S * WB;
    static constexpr int NM = TN * TM;
    static_assert(WP % NW == 0, "W pieces per wave");
    static_assert(BN % 128 == 0 && TW % 32 == 0, "shape");
    static_assert(SH == 32 || TN % 2 == 0, "TN even for the 16x16 shape");
    static_assert((S - 2) * LG <= 63, "vmcnt range");
};

template <class C>
struct DGState {
    const char* wbase;
    unsigned woff[C::LW];
    const char* xbase;
    unsigned xoff[C::TM32];
    long wstride;
    char LDSP* lds;
    unsigned lds_u;
    unsigned wdst;
    unsigned a_off[2];
};
template <class C>
struct DGFrags {
    s16x4 alo[2][C::TN], ahi[2][C::TN];
    bf16x8 b[C::S][C::KS][C::TM];
};

DI void glds16_asm(unsigned lds_addr, const char* sbase, unsigned voff) {
    asm volatile("s_mov_b32 m0, %0\n\ts_nop 0\n\tglobal_load_lds_dwordx4 %1, %2" :: "s"(lds_addr), "v"(voff), "s"(sbase) : "memory");
}
template <class C>
DI void dg_glds(const DGState<C>& st, int g, int t, int slot) {
    glds16_asm(st.lds_u + slot * C::WB + st.wdst + g * 1024, st.wbase + (long)t * st.wstride, st.woff[g]);
}
template <bool AG> DI void mfma_asm(f32x4& acc, const bf16x8& a, const bf16x8& b) {
    if constexpr (AG) asm volatile("v_mfma_f32_16x16x32_bf16 %0, %1, %2, %0" : "+a"(acc) : "v"(a), "v"(b));
    else asm volatile("v_mfma_f32_16x16x32_bf16 %0, %1, %2, %0" : "+v"(acc) : "v"(a), "v"(b));
}
template <bool AG> DI void mfma_asm(f32x16& acc, const bf16x8& a, const bf16x8& b) {
    if constexpr (AG) asm volatile("v_mfma_f32_32x32x16_bf16 %0, %1, %2, %0" : "+a"(acc) : "v"(a), "v"(b));
    else asm volatile("v_mfma_f32_32x32x16_bf16 %0, %1, %2, %0" : "+v"(acc) : "v"(a), "v"(b));
}
template <bool AG, class T, int TN, int TM>
DI void dg_zero_acc(T (&acc)[TN][TM]) {
#pragma unroll
    for (int a = 0; a < TN; ++a)
#pragma unroll
        for (int b = 0; b < TM; ++b) {
            acc[a][b] = (T){};
            if constexpr (AG) asm volatile("" : "+a"(acc[a][b]));
            else asm volatile("" : "+v"(acc[a][b]));
        }
    asm volatile("s_nop 1");
}
template <bool AG, class T, int TN, int TM>
DI void dg_drain_mfma(T (&acc)[TN][TM]) {
    asm volatile("s_nop 15");
#pragma unroll
    for (int a = 0; a < TN; ++a)
#pragma unroll
        for (int b = 0; b < TM; ++b) {
            if constexpr (AG) asm volatile("" : "+a"(acc[a][b]));
            else asm volatile("" : "+v"(acc[a][b]));
        }
    __builtin_amdgcn_sched_barrier(0);
}
template <int IMM>
DI void gload16_asm(bf16x8& dst, const char* sbase, unsigned voff) {
    asm volatile("global_load_dwordx4 %0, %1, %2 offset:%3" : "=v"(dst) : "v"(voff), "s"(sbase), "n"(IMM) : "memory");
}
template <class C, int BUF, int D>
DI void dg_bload(const DGState<C>& st, int t, DGFrags<C>& f) {
    constexpr int ks = D / C::TM, tb = D % C::TM;
    const char* sb = st.xbase + (long)t * 4096;
    if constexpr (C::SH == 32) gload16_asm<ks * 1024>(f.b[BUF][ks][tb], sb, st.xoff[tb]);
    else gload16_asm<(tb & 1) * 256 + ks * 2048>(f.b[BUF][ks][tb], sb, st.xoff[tb >> 1]);
}
template <class C>
DI void dg_aread(const DGState<C>& st, int k, int slot, int ks, int sel, DGFrags<C>& f) {
    const int ta = k >> 1, half = k & 1;
    const char LDSP* p;
    if constexpr (C::SH == 32) p = st.lds + (slot * C::WB + ((4 * ks + half) * C::NTL + ta) * 256) + st.a_off[0];
    else p = st.lds + (slot * C::WB + ((8 * ks + half) * C::NTL + (ta >> 1)) * 256) + st.a_off[ta & 1];
    if (half) f.ahi[sel][ta] = tr_read(p); else f.alo[sel][ta] = tr_read(p);
}
#define DG_DRAIN 0
template <class C, int N0, int BUF>
DI void dg_wait(DGFrags<C>& f) {
    constexpr int N = DG_DRAIN ? 0 : N0;
    if constexpr (C::LB == 4) {
        constexpr int T = C::TM;
        asm volatile("s_waitcnt vmcnt(%4) lgkmcnt(0)"
                     : "+v"(f.b[BUF][0 / T][0 % T]), "+v"(f.b[BUF][1 / T][1 % T]), "+v"(f.b[BUF][2 / T][2 % T]), "+v"(f.b[BUF][3 / T][3 % T])
                     : "n"(N) : "memory");
    } else {
        static_assert(C::LB == 8, "LB");
        constexpr int T = C::TM;
        asm volatile("s_waitcnt vmcnt(%8) lgkmcnt(0)"
                     : "+v"(f.b[BUF][0 / T][0 % T]), "+v"(f.b[BUF][1 / T][1 % T]), "+v"(f.b[BUF][2 / T][2 % T]), "+v"(f.b[BUF][3 / T][3 % T]),
                       "+v"(f.b[BUF][4 / T][4 % T]), "+v"(f.b[BUF][5 / T][5 % T]), "+v"(f.b[BUF][6 / T][6 % T]), "+v"(f.b[BUF][7 / T][7 % T])
                     : "n"(N) : "memory");
    }
}
template <class C> constexpr int dg_aslot(int k) {
    return k * (C::NM - (C::NM / 4 > 1 ? C::NM / 4 : 1)) / (2 * C::TN);
}

template <class C, bool AG, bool TAIL, int B, int KSI, int EXTRA = 0>
DI void dg_step(const DGState<C>& st, int t, typename AccT<C::SH>::t (&acc)[C::TN][C::TM], DGFrags<C>& f) {
    constexpr int cur = (B * C::KS + KSI) & 1, nxt = cur ^ 1;
    constexpr bool LAST = KSI == C::KS - 1, FIRST = KSI == 0;
    constexpr bool HAS_NEXT = !(LAST && TAIL && B == C::S - 1);
    constexpr int nslot = LAST ? (B + 1) % C::S : B, nks = LAST ? 0 : KSI + 1;
    constexpr bool HAS_P = FIRST && (!TAIL || B == 0);
    constexpr int pslot = (B + C::S - 1) % C::S;
    if constexpr (LAST && HAS_NEXT) {
        dg_wait<C, (TAIL ? C::S - B - 2 : C::S - 2) * C::LG + EXTRA, (B + 1) % C::S>(f);
        wg_barrier();
    }
    static_for<0, C::NM>([&](auto ic) {
        constexpr int i = decltype(ic)::value, ta = i / C::TM, tb = i % C::TM;
        mfma_asm<AG>(acc[ta][tb], cat8(f.alo[cur][ta], f.ahi[cur][ta]), f.b[B][KSI][tb]);
        if constexpr (HAS_NEXT) {
            static_for<0, 2 * C::TN>([&](auto kc) {
                constexpr int k = decltype(kc)::value;
                if constexpr (dg_aslot<C>(k) == i) dg_aread<C>(st, k, nslot, nks, nxt, f);
            });
        }
        if constexpr (HAS_P) {
            static_for<0, C::LB>([&](auto dc) {
                constexpr int d = decltype(dc)::value;
                if constexpr (d * C::NM / C::LG == i) dg_bload<C, pslot, d>(st, t + C::S - 1, f);
            });
            static_for<0, C::LW>([&](auto gc) {
                constexpr int g = decltype(gc)::value;
                if constexpr ((C::LB + g) * C::NM / C::LG == i) dg_glds<C>(st, g, t + C::S - 1, pslot);
            });
        }
        __builtin_amdgcn_sched_barrier(0);
    });
}
template <class C, bool AG, bool TAIL, int B, int EXTRA = 0, int KSI = 0>
DI void dg_stage(const DGState<C>& st, int t, typename AccT<C::SH>::t (&acc)[C::TN][C::TM], DGFrags<C>& f) {
    dg_step<C, AG, TAIL, B, KSI, EXTRA>(st, t, acc, f);
    if constexpr (KSI + 1 < C::KS) dg_stage<C, AG, TAIL, B, EXTRA, KSI + 1>(st, t, acc, f);
}
template <class C, bool AG, bool TAIL, int B = 0>
DI void dg_group(const DGState<C>& st, int t0, typename AccT<C::SH>::t (&acc)[C::TN][C::TM], DGFrags<C>& f) {
    dg_stage<C, AG, TAIL, B>(st, t0 + B, acc, f);
    if constexpr (B + 1 < C::S) dg_group<C, AG, TAIL, B + 1>(st, t0, acc, f);
}

template <class C>
DI void dg_setup(DGState<C>& st, const char* Xw, int xks, const char* Wg, int ldw, char LDSP* lds) {
    const int tid = threadIdx.x, lane = tid & 63;
    const int wid = __builtin_amdgcn_readfirstlane(tid >> 6);
    st.lds = lds;
    const int q4 = (lane >> 2) & 3;
#pragma unroll
    for (int j = 0; j < C::LW; ++j) {
        const int pc = wid * C::LW + j, kq = pc / (C::BN / 128), a = pc % (C::BN / 128);
        const int cc = C::SH == 32 ? (lane & 3) : ((lane & 3) ^ (2 * ((kq >> 1) & 1)));
        st.woff[j] = (unsigned)(((4 * kq + q4) * ldw + 128 * a + 32 * (lane >> 4) + 8 * cc) * 2);
    }
    st.wbase = Wg;
    st.lds_u = (unsigned)(size_t)lds;
    st.wstride = (long)C::BK * ldw * 2;
    st.wdst = wid * C::LW * 1024;
    st.xbase = Xw;
    if constexpr (C::SH == 32) {
#pragma unroll
        for (int i = 0; i < C::TM32; ++i) st.xoff[i] = lane * 16 + i * xks * 1024;
        const int h = lane >> 5, gg = (lane >> 4) & 1, p = lane & 3, q4r = (lane & 15) >> 2;
        const int n4 = 4 * (p & 1) + 2 * gg + (p >> 1);
        st.a_off[0] = st.a_off[1] = (2 * h * C::NTL) * 256 + q4r * 64 + n4 * 8;
    } else {
        const int g = lane >> 4, p = lane & 3, q4r = (lane & 15) >> 2;
#pragma unroll
        for (int i = 0; i < C::TM32; ++i) st.xoff[i] = (g >> 1) * 1024 + (g & 1) * 512 + (lane & 15) * 16 + i * xks * 1024;
        const int base = (2 * g * C::NTL) * 256 + q4r * 64 + p * 8;
        st.a_off[0] = base + (g & 1) * 32;
        st.a_off[1] = base + ((g & 1) ^ 1) * 32;
    }
}
template <class C>
DI void dg_prologue(const DGState<C>& st, DGFrags<C>& f) {
    static_for<0, C::S - 1>([&](auto ic) {
        constexpr int i = decltype(ic)::value;
        static_for<0, C::LB>([&](auto dc) { dg_bload<C, i, decltype(dc)::value>(st, i, f); });
#pragma unroll
        for (int g = 0; g < C::LW; ++g) dg_glds<C>(st, g, i, i);
    });
}
template <class C, int EXTRA = 0>
DI void dg_start(const DGState<C>& st, DGFrags<C>& f) {
    dg_wait<C, (C::S - 2) * C::LG + EXTRA, 0>(f);
    wg_barrier();
    static_for<0, 2 * C::TN>([&](auto kc) { dg_aread<C>(st, decltype(kc)::value, 0, 0, 0, f); });
    __builtin_amdgcn_sched_barrier(0);
}
template <class C, bool AG>
DI void dg_run(const DGState<C>& st, int nt, typename AccT<C::SH>::t (&acc)[C::TN][C::TM], DGFrags<C>& f) {
    dg_start<C>(st, f);
    int t0 = 0;
    for (; t0 < nt - C::S; t0 += C::S) dg_group<C, AG, false>(st, t0, acc, f);
    dg_group<C, AG, true>(st, t0, acc, f);
    dg_drain_mfma<AG>(acc);
}

using K2Q = DC<256, 128, 8, 4, 32>;
using K2F = DC<256, 128, 8, 4, 16>;
#define K2_ABL 0
#define K2_WARM 7
#define K2_WARM_A ((K2_WARM) & 1)
#define K2_WARM_B ((K2_WARM) & 2)
#define K2_WARM_C ((K2_WARM) & 4)
constexpr int K2_LDS = 160 * 1024;
constexpr int K2_TB = 16384;
DI constexpr int k2_kslot(int j) { return j % 3 == 0 ? 6 : (j % 3 == 1 ? 7 : 4); }
DI constexpr int k2_vslot(int j) { return (j & 3) == 0 ? 8 : ((j & 3) == 1 ? 9 : ((j & 3) == 2 ? 5 : 3)); }
constexpr float K2_C = 0.088388347648318440f * 1.4426950408889634f;

struct K2Attn {
    const char* kbase; const char* vbase;
    int wid;
    unsigned lds_u, dst;
    char LDSP* lds;
    unsigned k_addr, v_addrA, v_addrB;
};
DI void k2_issue_k(const K2Attn& a, int j) {
    int tz = threadIdx.x; asm volatile("" : "+v"(tz));
    const int lane = tz & 63;
#pragma unroll
    for (int i = 0; i < 2; ++i) {
        const int key = 4 * (a.wid * 2 + i) + (lane >> 4), c = (lane & 15) ^ (key & 15);
        glds16_asm(a.lds_u + k2_kslot(j) * K2_TB + a.dst + i * 1024, a.kbase + (long)j * K2_TB, (unsigned)(key * 256 + c * 16));
    }
}
DI void k2_issue_v(const K2Attn& a, int j) {
    int tz = threadIdx.x; asm volatile("" : "+v"(tz));
    const int lane = tz & 63;
#pragma unroll
    for (int i = 0; i < 2; ++i) {
        const int kv = 4 * (a.wid * 2 + i) + ((lane >> 2) & 3);
        glds16_asm(a.lds_u + k2_vslot(j) * K2_TB + a.dst + i * 1024, a.vbase + (long)j * K2_TB, (unsigned)(kv * 256 + ((lane >> 4) * 32 + (lane & 3) * 8) * 2));
    }
}
template <int J, int I>
DI bf16x8 k2_kfrag(const K2Attn& a) {
    constexpr int u = I >> 1, kt = I & 1, cu = 4 * (u >> 1) + (u & 1);
    return *(const bf16x8 LDSP*)(a.lds + ((a.k_addr ^ (unsigned)(cu << 4)) + (unsigned)((k2_kslot(J) - 4) * K2_TB + kt * 8192)));
}
template <int J, int I, int HALF>
DI s16x4 k2_vfrag(const K2Attn& a) {
    constexpr int t = I & 3, ks = I >> 2, sl = k2_vslot(J);
    constexpr int off = ((4 * ks + 2 * HALF) * 4 + t) * 256;
    if constexpr (sl == 3 || sl == 5) return tr_read(a.lds + ((sl - 3) * K2_TB + off) + a.v_addrB);
    else return tr_read(a.lds + ((sl - 8) * K2_TB + off) + a.v_addrA);
}
template <int N> DI void wait_vm() { asm volatile("s_waitcnt vmcnt(%0)" ::"n"(N) : "memory"); }

struct K2Soft {
    f32x16 o[4];
    float m_run, l_run, alpha, mc, ps, tm;
    u32x4 pf[2][4];
    f32x16 T[3];
    bf16x8 kfr[4];
    s16x4 vlo[4], vhi[4];
};
DI float max3_asm(float a, float b, float c) { float r; asm volatile("v_max3_f32 %0, %1, %2, %3" : "=v"(r) : "v"(a), "v"(b), "v"(c)); return r; }
template <class T> DI void anchor(T& x) { asm volatile("" : "+v"(x)); }
template <int PAR, int F, int W>
DI void k2_exp4(K2Soft& z, float s0, float s1, float s2, float s3) {
    const float p0 = __builtin_amdgcn_exp2f(fmaf(s0, K2_C, z.mc)), p1 = __builtin_amdgcn_exp2f(fmaf(s1, K2_C, z.mc));
    const float p2 = __builtin_amdgcn_exp2f(fmaf(s2, K2_C, z.mc)), p3 = __builtin_amdgcn_exp2f(fmaf(s3, K2_C, z.mc));
    z.ps += (p0 + p1) + (p2 + p3);
    unsigned k0 = pk_bf16(p0, p1), k1 = pk_bf16(p2, p3);
    anchor(z.ps); anchor(k0); anchor(k1);
    z.pf[PAR][F][W] = k0;
    z.pf[PAR][F][W + 1] = k1;
}
template <int j, int c>
DI void k2_sm_start(K2Soft& z) {
    f32x16& sa = z.T[(2 * j) % 3]; f32x16& sb = z.T[(2 * j + 1) % 3];
    if constexpr (c < 4) {
        float t = c == 0 ? max3_asm(sa[0], sa[1], sa[2]) : max3_asm(z.tm, sa[4 * c], sa[4 * c + 1]);
        t = c == 0 ? max3_asm(t, sa[3], sa[3]) : max3_asm(t, sa[4 * c + 2], sa[4 * c + 3]);
        t = max3_asm(t, sb[4 * c], sb[4 * c + 1]);
        z.tm = max3_asm(t, sb[4 * c + 2], sb[4 * c + 3]);
    } else if constexpr (c == 4) {
        auto rr = __builtin_amdgcn_permlane32_swap(__float_as_uint(z.tm), __float_as_uint(z.tm), false, false);
        const float m_new = fmaxf(z.m_run, fmaxf(__uint_as_float(rr[0]), __uint_as_float(rr[1])));
        z.alpha = __builtin_amdgcn_exp2f((z.m_run - m_new) * K2_C);
        z.m_run = m_new;
        z.mc = -m_new * K2_C;
        z.ps = 0.f;
        anchor(z.alpha); anchor(z.mc);
    } else if constexpr (c == 5 || c == 7 || c == 9 || c == 11) {
        constexpr int k = (c - 5) / 2;
        k2_exp4<j & 1, (k >> 1), 2 * (k & 1)>(z, sa[4 * k], sa[4 * k + 1], sa[4 * k + 2], sa[4 * k + 3]);
    }
}
template <int j, int c>
DI void k2_sm_finish(K2Soft& z) {
    f32x16& sb = z.T[(2 * j + 1) % 3];
    if constexpr (c == 0 || c == 2 || c == 4 || c == 6) {
        constexpr int k = c / 2;
        k2_exp4<j & 1, 2 + (k >> 1), 2 * (k & 1)>(z, sb[4 * k], sb[4 * k + 1], sb[4 * k + 2], sb[4 * k + 3]);
    } else if constexpr (c == 8) {
        z.l_run = z.l_run * z.alpha + z.ps;
        anchor(z.l_run);
    }
}
template <int j>
DI void k2_phaseA(const K2Attn& a, const bf16x8 (&qf)[8], K2Soft& z) {
    constexpr int PFK = 3;
    f32x16& sa = z.T[(2 * j) % 3]; f32x16& sb = z.T[(2 * j + 1) % 3];
    if constexpr (j < 8) {
        static_for<0, PFK>([&](auto ic) { constexpr int i = decltype(ic)::value; z.kfr[i & 3] = k2_kfrag<(j < 8 ? j : 0), i>(a); });
        __builtin_amdgcn_sched_barrier(0);
    }
    static_for<0, 16>([&](auto ic) {
        constexpr int i = decltype(ic)::value;
        if constexpr (j < 8) {
            if constexpr (i == 0) sa = MFMA32(z.kfr[0], qf[0], (f32x16){});
            else if constexpr (i == 1) sb = MFMA32(z.kfr[1], qf[0], (f32x16){});
            else if constexpr (i & 1) sb = MFMA32(z.kfr[i & 3], qf[i >> 1], sb);
            else sa = MFMA32(z.kfr[i & 3], qf[i >> 1], sa);
#if !(K2_ABL & 2)
            if constexpr (i + PFK < 16) z.kfr[(i + PFK) & 3] = k2_kfrag<(j < 8 ? j : 0), (i + PFK < 16 ? i + PFK : 0)>(a);
#endif
        }
#if !(K2_ABL & 1)
        if constexpr (j >= 1) k2_sm_finish<j - 1, i>(z);
#endif
        if constexpr (j >= 2 && !(K2_ABL & 4)) {
#pragma unroll
            for (int u = 0; u < 4; ++u) { float v = z.o[i >> 2][4 * (i & 3) + u] * z.alpha; anchor(v); z.o[i >> 2][4 * (i & 3) + u] = v; }
        }
        __builtin_amdgcn_sched_barrier(0);
    });
}
template <int j>
DI void k2_phaseB(const K2Attn& a, K2Soft& z) {
    constexpr int PFV = 2;
    if constexpr (j >= 1) {
        static_for<0, PFV>([&](auto ic) { constexpr int i = decltype(ic)::value;
            z.vlo[i & 3] = k2_vfrag<(j >= 1 ? j - 1 : 0), i, 0>(a); z.vhi[i & 3] = k2_vfrag<(j >= 1 ? j - 1 : 0), i, 1>(a); });
    }
    if constexpr (j < 8) asm volatile("s_nop 11");
    __builtin_amdgcn_sched_barrier(0);
    static_for<0, 16>([&](auto ic) {
        constexpr int i = decltype(ic)::value;
        if constexpr (j >= 1) {
            z.o[i & 3] = MFMA32(cat8(z.vlo[i & 3], z.vhi[i & 3]), __builtin_bit_cast(bf16x8, z.pf[(j - 1) & 1][i >> 2]), z.o[i & 3]);
#if !(K2_ABL & 2)
            if constexpr (i + PFV < 16) {
                z.vlo[(i + PFV) & 3] = k2_vfrag<(j >= 1 ? j - 1 : 0), (i + PFV < 16 ? i + PFV : 0), 0>(a);
                z.vhi[(i + PFV) & 3] = k2_vfrag<(j >= 1 ? j - 1 : 0), (i + PFV < 16 ? i + PFV : 0), 1>(a);
            }
#endif
        }
#if !(K2_ABL & 1)
        if constexpr (j < 8) k2_sm_start<(j < 8 ? j : 0), i>(z);
#else
        if constexpr (j < 8 && i == 5) {
#pragma unroll
            for (int f = 0; f < 4; ++f)
#pragma unroll
                for (int w = 0; w < 4; ++w) z.pf[j & 1][f][w] = __float_as_uint(f < 2 ? z.T[(2 * j) % 3][4 * f + w] : z.T[(2 * j + 1) % 3][4 * f + w]);
        }
#endif
        __builtin_amdgcn_sched_barrier(0);
    });
}

constexpr int QF_S = 3, QF_WB = 32768, QF_PF = 4, QF_R = 6, QF_RB = 6, QF_PB = 5, QF_NT = 16;
struct QFState {
    const char* bq; const char* bf;
    unsigned woff[4];
    const char* xbase; unsigned xoff;
    unsigned lds_u, wdst, a_off; char LDSP* lds;
};
struct QFFrags { s16x4 alo[QF_R], ahi[QF_R]; bf16x8 b[QF_RB]; };
constexpr int qf_hook(int T) { return T <= 3 ? 2 : 0; }
constexpr int qf_younger(int type, int idx, int stopT, int stopN, int warm) {
    int cnt = 0; bool seen = false;
    for (int g = 0; g < 4; ++g) { if (seen) ++cnt; }
    if (type == 0 && idx == 0) seen = true;
    for (int q = 0; q < QF_PB; ++q) { if (seen) ++cnt; if (type == 1 && idx == q) seen = true; }
    for (int g = 0; g < 4; ++g) { if (seen) ++cnt; }
    if (type == 0 && idx == 1) seen = true;
    for (int w = 0; w < warm; ++w) { if (seen) ++cnt; }
    for (int T = 0; T < QF_NT; ++T) {
        for (int n = 0; n < 32; ++n) {
            if (T == stopT && n == stopN) return cnt;
            if (n % 8 == 0 && 4 * T + n / 8 + QF_PB < 4 * QF_NT) { if (seen) ++cnt; if (type == 1 && idx == 4 * T + n / 8 + QF_PB) seen = true; }
            if (n >= 1 && n <= 4 && T + 2 < QF_NT) { if (seen) ++cnt; if (n == 4 && type == 0 && idx == T + 2) seen = true; }
        }
        for (int h = 0; h < qf_hook(T); ++h) { if (seen) ++cnt; }
    }
    return cnt;
}
DI void qf_glds(const QFState& st, int g, int t) {
    glds16_asm(st.lds_u + (t % QF_S) * QF_WB + st.wdst + g * 1024, (g & 1) ? st.bf + (long)t * (64 * 7168) : st.bq + (long)t * (64 * 8192), st.woff[g]);
}
template <int Q> DI void qf_bload(const QFState& st, QFFrags& f) { gload16_asm<(Q & 3) * 1024>(f.b[Q % QF_RB], st.xbase + (long)(Q >> 2) * 4096, st.xoff); }
template <int T, int M> DI void qf_aread(const QFState& st, QFFrags& f) {
    constexpr int ks = M >> 3, ta = M & 7, r = (32 * T + M) % QF_R;
    const char LDSP* p = st.lds + ((T % QF_S) * QF_WB + ((4 * ks) * 8 + ta) * 256) + st.a_off;
    f.alo[r] = tr_read(p);
    f.ahi[r] = tr_read(p + 8 * 256);
}
template <int T, int WARM>
DI void qf_stage(const QFState& st, f32x16 (&acc)[8][1], QFFrags& f) {
    static_for<0, 32>([&](auto nc) {
        constexpr int n = decltype(nc)::value, q = 4 * T + (n >> 3), r = (32 * T + n) % QF_R;
        if constexpr (n % 8 == 0) {
            asm volatile("s_waitcnt vmcnt(%1)" : "+v"(f.b[q % QF_RB]) : "n"(qf_younger(1, q, T, n, WARM)) : "memory");
        }
        mfma_asm<false>(acc[n & 7][0], cat8(f.alo[r], f.ahi[r]), f.b[q % QF_RB]);
        if constexpr (n + QF_PF < 32) qf_aread<T, (n + QF_PF < 32 ? n + QF_PF : 0)>(st, f);
        else if constexpr (T + 1 < QF_NT) qf_aread<T + 1, (n + QF_PF - 32)>(st, f);
        if constexpr (n % 8 == 0 && q + QF_PB < 4 * QF_NT) qf_bload<(q + QF_PB < 4 * QF_NT ? q + QF_PB : 0)>(st, f);
        if constexpr (n >= 1 && n <= 4 && T + 2 < QF_NT) qf_glds(st, n - 1, T + 2);
        if constexpr (n == 31 - QF_PF && T + 1 < QF_NT) {
            asm volatile("s_waitcnt vmcnt(%0) lgkmcnt(0)" :: "n"(qf_younger(0, T + 1, T, n + 1, WARM)) : "memory");
            wg_barrier();
        }
        __builtin_amdgcn_sched_barrier(0);
    });
}

__global__ __launch_bounds__(512, 2) void k2_kernel(const u16* __restrict__ xn, const u16* __restrict__ wq, const u16* __restrict__ kc, const u16* __restrict__ vc,
                                                    const u16* __restrict__ w1, u16* __restrict__ o_out, u16* __restrict__ h_out,
                                                    const u16* __restrict__ wo, const u16* __restrict__ w2
                                                    ) {
    __shared__ __attribute__((aligned(1024))) char smem[K2_LDS];
    TL_START(1);
    const int bid = blockIdx.x;
    const int head = (bid & 7) * 4 + ((bid >> 3) & 3), qb = bid >> 5;
    const int wid = __builtin_amdgcn_readfirstlane(threadIdx.x >> 6);
    char LDSP* lds = (char LDSP*)smem;
#define K2_TS(i)
    const char* Xw = (const char*)xn + (size_t)(qb * 8 + wid) * 64 * 1024;
    const bool hasF = head < 28;
    K2Attn a;
    {
        const int kvh = head >> 3;
        a.kbase = (const char*)(kc + ((size_t)kvh * 2048 + 1536) * 128);
        a.vbase = (const char*)(vc + ((size_t)kvh * 2048 + 1536) * 128);
        a.lds = lds; a.lds_u = (unsigned)(size_t)lds; a.dst = wid * 2048; a.wid = wid;
    }
    bf16x8 qf[8];
    auto pack_q = [&](auto& acc) {
#pragma unroll
        for (int u = 0; u < 8; ++u) {
            u32x4 w;
#pragma unroll
            for (int i = 0; i < 4; ++i) w[i] = pk_bf16(acc[u >> 1][0][8 * (u & 1) + 2 * i], acc[u >> 1][0][8 * (u & 1) + 2 * i + 1]);
            qf[u] = __builtin_bit_cast(bf16x8, w);
        }
    };
    if (hasF) {
        f32x16 acc[8][1];
        dg_zero_acc<false>(acc);
        QFState st; QFFrags f;
        {
            const int lane = threadIdx.x & 63, q4 = (lane >> 2) & 3;
            st.bq = (const char*)(wq + head * 128); st.bf = (const char*)(w1 + head * 128);
#pragma unroll
            for (int g = 0; g < 4; ++g)
                st.woff[g] = (unsigned)(((4 * (2 * wid + (g >> 1)) + q4) * ((g & 1) ? 3584 : 4096) + 32 * (lane >> 4) + 8 * (lane & 3)) * 2);
            st.xbase = Xw; st.xoff = lane * 16;
            st.lds = lds; st.lds_u = (unsigned)(size_t)lds; st.wdst = wid * 4096;
            const int h = lane >> 5, gg = (lane >> 4) & 1, p = lane & 3, q4r = (lane & 15) >> 2;
            st.a_off = (2 * h * 8) * 256 + q4r * 64 + (4 * (p & 1) + 2 * gg + (p >> 1)) * 8;
        }
#pragma unroll
        for (int g = 0; g < 4; ++g) qf_glds(st, g, 0);
        static_for<0, QF_PB>([&](auto qc) { qf_bload<decltype(qc)::value>(st, f); });
#pragma unroll
        for (int g = 0; g < 4; ++g) qf_glds(st, g, 1);
        {
#pragma unroll
            for (int u = 0; u < 2; ++u) {
                const int sg = 2 + 2 * qb + u < 15 ? 2 + 2 * qb + u : 15;
#pragma unroll
                for (int g = 0; g < 4; ++g)
                    glds16_asm(st.lds_u + 9 * K2_TB + a.dst + (g & 1) * 1024, (g & 1) ? st.bf + (long)sg * (64 * 7168) : st.bq + (long)sg * (64 * 8192), st.woff[g]);
            }
        }
        constexpr int WARM = K2_WARM_A ? 8 : 0;
        asm volatile("s_waitcnt vmcnt(%0)" :: "n"(qf_younger(0, 0, 0, 0, WARM)) : "memory");
        wg_barrier();
        static_for<0, QF_PF>([&](auto mc) { qf_aread<0, decltype(mc)::value>(st, f); });
        __builtin_amdgcn_sched_barrier(0);
        K2_TS(1);
        static_for<0, 16>([&](auto tc) {
            constexpr int t = decltype(tc)::value;
            qf_stage<t, WARM>(st, acc, f);
            if constexpr (t == 0) k2_issue_k(a, 0);
            if constexpr (t == 1) k2_issue_v(a, 0);
            if constexpr (t == 2) k2_issue_k(a, 1);
            if constexpr (t == 3) k2_issue_v(a, 1);
        });
        dg_drain_mfma<false>(acc);
        K2_TS(2);
        {
            int tz = threadIdx.x; asm volatile("" : "+v"(tz));
            const __amdgpu_buffer_rsrc_t hrs = __builtin_amdgcn_make_buffer_rsrc(h_out, 0, 2048u * 3584u * 2u, 0x00020000);
            const unsigned hbase = (unsigned)(((qb * 8 + wid) * 224 + head * 8) * 1024);
#pragma unroll
            for (int t = 0; t < 4; ++t) {
#pragma unroll
                for (int r = 0; r < 16; ++r) acc[4 + t][0][r] = fmaxf(acc[4 + t][0][r], 0.f);
                store_tile_frag_wt(hrs, hbase + t * 2048, acc[4 + t][0], tz & 63);
            }
        }
        pack_q(acc);
    } else {
        f32x16 accq[4][1];
        dg_zero_acc<false>(accq);
        DGState<K2Q> st; DGFrags<K2Q> f;
        dg_setup<K2Q>(st, Xw, 64, (const char*)(wq + head * 128), 4096, lds);
        dg_prologue<K2Q>(st, f);
        dg_start<K2Q>(st, f);
        K2_TS(1);
        static_for<0, 16>([&](auto tc) {
            constexpr int t = decltype(tc)::value;
            constexpr auto hook = [](int u) { return (u >= 0 && u <= 3) ? 2 : 0; };
            dg_stage<K2Q, false, (t >= 12), (t & 3), hook(t - 1) + hook(t - 2)>(st, t, accq, f);
            if constexpr (t == 0) k2_issue_k(a, 0);
            if constexpr (t == 1) k2_issue_v(a, 0);
            if constexpr (t == 2) k2_issue_k(a, 1);
            if constexpr (t == 3) k2_issue_v(a, 1);
        });
        dg_drain_mfma<false>(accq);
        K2_TS(2);
        pack_q(accq);
    }
    {
        int tz = threadIdx.x; asm volatile("" : "+v"(tz));
        const int lane = tz & 63, h = lane >> 5;
        a.k_addr = 4 * K2_TB + (lane & 31) * 256 + ((((2 * h) ^ (lane & 15)) & 15) << 4);
        const int gg = (lane >> 4) & 1, p = lane & 3, q4 = (lane & 15) >> 2;
        const int n4 = 4 * (p & 1) + 2 * gg + (p >> 1);
        const unsigned vl = h * 4 * 256 + q4 * 64 + n4 * 8;
        a.v_addrA = 8 * K2_TB + vl; a.v_addrB = 3 * K2_TB + vl;
        asm volatile("" : "+v"(a.k_addr), "+v"(a.v_addrA), "+v"(a.v_addrB));
    }
    K2Soft z;
#pragma unroll
    for (int t = 0; t < 4; ++t) z.o[t] = (f32x16){};
    z.m_run = -INFINITY; z.l_run = 0.f; z.alpha = 1.f; z.mc = 0.f; z.ps = 0.f;
    auto top = [&](auto ic) {
        constexpr int i = decltype(ic)::value;
        if constexpr (i >= 2) wait_vm<(i - 1 <= 5 ? 4 : (i - 1 == 6 ? 2 : 0))>();
        wg_barrier();
        if constexpr (i <= 5) k2_issue_k(a, i + 2);
        if constexpr (i >= 1 && i <= 6) k2_issue_v(a, i + 1);
        if constexpr (i < 8) {
            int pc = (bid * 8 + wid) * 8 + i;
            pc = pc < 15360 ? pc : 15359;
            const char* src = pc < 8192 ? (const char*)wo + (size_t)pc * 1024 : (const char*)w2 + (size_t)(pc - 8192) * 1024;
            int tz = threadIdx.x; asm volatile("" : "+v"(tz));
            glds16_asm(a.lds_u + K2_TB + wid * 1024, src, (unsigned)(tz & 63) * 16);
        }
    };
#define K2_STAMP(n)
    if (wid < 4) {
        top(std::integral_constant<int, 0>{});
        static_for<0, 9>([&](auto jc) {
            constexpr int j = decltype(jc)::value;
            K2_STAMP(4 * j);
            if constexpr (j >= 1) top(jc);
            K2_STAMP(4 * j + 1);
            k2_phaseA<j>(a, qf, z);
            K2_STAMP(4 * j + 2);
            k2_phaseB<j>(a, z);
            K2_STAMP(4 * j + 3);
        });
    } else {
        top(std::integral_constant<int, 0>{});
        static_for<0, 9>([&](auto jc) {
            constexpr int j = decltype(jc)::value;
            K2_STAMP(4 * j + 1);
            k2_phaseA<j>(a, qf, z);
            K2_STAMP(4 * j + 2);
            if constexpr (j < 8) top(std::integral_constant<int, j + 1>{});
            K2_STAMP(4 * j);
            k2_phaseB<j>(a, z);
            K2_STAMP(4 * j + 3);
        });
    }
    K2_TS(3);
    {
        int tz = threadIdx.x; asm volatile("" : "+v"(tz));
        const int lane = tz & 63;
        auto rr = __builtin_amdgcn_permlane32_swap(__float_as_uint(z.l_run), __float_as_uint(z.l_run), false, false);
        const float inv = 1.0f / (__uint_as_float(rr[0]) + __uint_as_float(rr[1]));
        const __amdgpu_buffer_rsrc_t ors = __builtin_amdgcn_make_buffer_rsrc(o_out, 0, 2048u * 4096u * 2u, 0x00020000);
        const unsigned obase = (unsigned)(((qb * 8 + wid) * 256 + head * 8) * 1024);
#pragma unroll
        for (int t = 0; t < 4; ++t) {
#pragma unroll
            for (int r = 0; r < 16; ++r) z.o[t][r] *= inv;
            store_tile_frag_wt(ors, obase + t * 2048, z.o[t], lane);
        }
    }
    K2_TS(4);
    TL_END(1);
}

using K3C = DC<128, 128, 4, 4, 16>;
constexpr int K3_STG_PITCH = 272;
constexpr int K3_STG_WAVE = 32 * K3_STG_PITCH;
constexpr int K3_LDS = K3C::LDS_BYTES + 4 * K3_STG_WAVE + 16;

typedef __attribute__((address_space(1))) unsigned gu32;
DI unsigned atomic_load_agent(unsigned* p) { return __hip_atomic_load(p, __ATOMIC_RELAXED, __HIP_MEMORY_SCOPE_AGENT); }
DI unsigned atomic_add_agent(unsigned* p, unsigned v) { return __hip_atomic_fetch_add(p, v, __ATOMIC_RELAXED, __HIP_MEMORY_SCOPE_AGENT); }

DI float silu_bf(float a) {
    const float x = rbf(a);
    const float e = __builtin_amdgcn_exp2f(-x * 1.4426950408889634f);
    return rbf(x * __builtin_amdgcn_rcpf(1.0f + e));
}

DI bool k3_exchange(const u32x4 (&mine)[8], u32x4 (&theirs)[8], unsigned c0, unsigned* cnt, char* my_slot, char* their_slot, int lane, int wid, volatile unsigned LDSP* word) {
    if (wid == 0) {
        if (lane == 0) *word = c0;
    }
    __syncthreads();
    unsigned c = __builtin_amdgcn_readfirstlane(*word);
    if (c == 0) {
        const __amdgpu_buffer_rsrc_t rs = __builtin_amdgcn_make_buffer_rsrc(my_slot, 0, 8192u, 0x00020000);
#pragma unroll
        for (int i = 0; i < 8; ++i) __builtin_amdgcn_raw_buffer_store_b128(mine[i], rs, i * 1024 + lane * 16, 0, 16  );
        asm volatile("s_waitcnt vmcnt(0)" ::: "memory");
        __syncthreads();
        if (wid == 0) {
            unsigned old = 0;
            if (lane == 0) old = atomic_add_agent(cnt, 1u);
            if (lane == 0) *word = old;
        }
        __syncthreads();
        c = __builtin_amdgcn_readfirstlane(*word);
        if (c == 0) return false;
    }
    const __amdgpu_buffer_rsrc_t rt = __builtin_amdgcn_make_buffer_rsrc(their_slot, 0, 8192u, 0x00020000);
#pragma unroll
    for (int i = 0; i < 8; ++i) theirs[i] = __builtin_amdgcn_raw_buffer_load_b128(rt, i * 1024 + lane * 16, 0, 16  );
    return true;
}

__global__ __launch_bounds__(256, 1) void k3_kernel(const u16* __restrict__ o_f, const u16* __restrict__ wo, const u16* __restrict__ h_f,
                                                    const u16* __restrict__ w2, const u16* __restrict__ x, u16* __restrict__ out,
                                                    char* __restrict__ exs, char* __restrict__ exg, unsigned* __restrict__ cnts
                                                    ) {
    using C = K3C;
    __shared__ __attribute__((aligned(1024))) char smem[K3_LDS];
    TL_START(2);
    const int bid = blockIdx.x, xcd = bid & 7, i5 = bid >> 3;
    const int type = xcd >> 2, pm = (xcd & 3) * 4 + (i5 & 3), pn = i5 >> 2;
    const int tile = pm * 8 + pn;
    const int wid = __builtin_amdgcn_readfirstlane(threadIdx.x >> 6);
    f32x4 acc[C::TN][C::TM];
    dg_zero_acc<true>(acc);
    const int Kd = type == 0 ? 4096 : 3584;
    {
        const char* Xw = (const char*)(type == 0 ? o_f : h_f) + (size_t)(pm * 4 + wid) * (Kd / 16) * 1024;
        const u16* W = (type == 0 ? wo : w2) + pn * 128;
        DGState<C> st; DGFrags<C> f;
        dg_setup<C>(st, Xw, Kd / 16, (const char*)W, 1024, (char LDSP*)smem);
        dg_prologue<C>(st, f);
        dg_run<C, true>(st, Kd / 64, acc, f);
    }
    int tz = threadIdx.x; asm volatile("" : "+v"(tz));
    const int lane = tz & 63;
    unsigned c0 = 0;
    if (wid == 0) c0 = atomic_load_agent(cnts + tile * 4);
    const size_t row0 = (size_t)pm * 128 + wid * 32;
    const u16* xp = x + (row0 + (lane >> 4)) * 1024 + pn * 128 + (lane & 15) * 8;
    u32x4 xv[8];
#pragma unroll
    for (int i = 0; i < 8; ++i) xv[i] = *(const u32x4*)(xp + (size_t)i * 4 * 1024);
    u32x4 mine[8], theirs[8];
#pragma unroll
    for (int i = 0; i < 8; ++i) {
        if (type == 0) mine[i] = (u32x4){pk_bf16(acc[i][0][0], acc[i][0][1]), pk_bf16(acc[i][0][2], acc[i][0][3]), pk_bf16(acc[i][1][0], acc[i][1][1]), pk_bf16(acc[i][1][2], acc[i][1][3])};
        else mine[i] = (u32x4){pk_bf16(silu_bf(acc[i][0][0]), silu_bf(acc[i][0][1])), pk_bf16(silu_bf(acc[i][0][2]), silu_bf(acc[i][0][3])),
                               pk_bf16(silu_bf(acc[i][1][0]), silu_bf(acc[i][1][1])), pk_bf16(silu_bf(acc[i][1][2]), silu_bf(acc[i][1][3]))};
    }
    const size_t slot = ((size_t)tile * 4 + wid) * 8192;
    if (!k3_exchange(mine, theirs, c0, cnts + tile * 4, (type == 0 ? exs : exg) + slot, (type == 0 ? exg : exs) + slot, lane, wid,
                     (volatile unsigned LDSP*)((char LDSP*)smem + K3_LDS - 16))) {
        TL_END(2);
        return;
    }
    char LDSP* stg = (char LDSP*)smem + C::LDS_BYTES + wid * K3_STG_WAVE;
#pragma unroll
    for (int i = 0; i < 8; ++i)
#pragma unroll
        for (int tb = 0; tb < 2; ++tb) {
            const unsigned a0 = mine[i][2 * tb], a1 = mine[i][2 * tb + 1], b0 = theirs[i][2 * tb], b1 = theirs[i][2 * tb + 1];
            const float p0 = __uint_as_float(a0 << 16) * __uint_as_float(b0 << 16), p1 = __uint_as_float(a0 & 0xffff0000u) * __uint_as_float(b0 & 0xffff0000u);
            const float p2 = __uint_as_float(a1 << 16) * __uint_as_float(b1 << 16), p3 = __uint_as_float(a1 & 0xffff0000u) * __uint_as_float(b1 & 0xffff0000u);
            *(u32x2 LDSP*)(stg + (16 * tb + (lane & 15)) * K3_STG_PITCH + (16 * i + 4 * (lane >> 4)) * 2) = (u32x2){pk_bf16(p0, p1), pk_bf16(p2, p3)};
        }
    u16* op = out + (row0 + (lane >> 4)) * 1024 + pn * 128 + (lane & 15) * 8;
#pragma unroll
    for (int i = 0; i < 8; ++i) {
        const u32x4 pv = *(const u32x4 LDSP*)(stg + (4 * i + (lane >> 4)) * K3_STG_PITCH + (lane & 15) * 16);
        u32x4 ov;
#pragma unroll
        for (int e = 0; e < 4; ++e)
            ov[e] = pk_bf16(__uint_as_float(xv[i][e] << 16) + __uint_as_float(pv[e] << 16), __uint_as_float(xv[i][e] & 0xffff0000u) + __uint_as_float(pv[e] & 0xffff0000u));
        *(u32x4*)(op + (size_t)i * 4 * 1024) = ov;
    }
    TL_END(2);
}

constexpr int M_ = 2048, D_ = 1024;

DI float wave_sum(float v) {
    int x = __float_as_int(v);
#define DPP_ADD(ctrl, rmask) x = __float_as_int(__int_as_float(x) + __int_as_float(__builtin_amdgcn_update_dpp(0, x, ctrl, rmask, 0xf, true)))
    DPP_ADD(0x111, 0xf); DPP_ADD(0x112, 0xf); DPP_ADD(0x114, 0xf); DPP_ADD(0x118, 0xf);
    DPP_ADD(0x142, 0xa); DPP_ADD(0x143, 0xc);
#undef DPP_ADD
    return __int_as_float(__builtin_amdgcn_readlane(x, 63));
}
__global__ __launch_bounds__(512) void ln_kernel(const u16* __restrict__ x, u16* __restrict__ xn, unsigned* __restrict__ cnts) {
    TL_START(0);
    const int lane = threadIdx.x & 63, wid = threadIdx.x >> 6;
    if (blockIdx.x == 0 && threadIdx.x < 128) cnts[threadIdx.x * 4] = 0u;
    const int row = blockIdx.x * 8 + wid;
    const u32x4* src = (const u32x4*)(x + (size_t)row * D_ + lane * 16);
    u32x4 a = src[0], b = src[1];
    float v[16];
#pragma unroll
    for (int i = 0; i < 4; ++i) { v[2 * i] = __uint_as_float(a[i] << 16); v[2 * i + 1] = __uint_as_float(a[i] & 0xffff0000u);
                                  v[8 + 2 * i] = __uint_as_float(b[i] << 16); v[8 + 2 * i + 1] = __uint_as_float(b[i] & 0xffff0000u); }
    float s = 0.f;
#pragma unroll
    for (int i = 0; i < 16; ++i) s += v[i];
    const float mean = wave_sum(s) * (1.0f / 1024.0f);
    float q = 0.f;
#pragma unroll
    for (int i = 0; i < 16; ++i) { const float d = v[i] - mean; q += d * d; }
    const float var = wave_sum(q) * (1.0f / 1024.0f);
    const float mb = rbf(mean), vb = rbf(var);
    const float den = rbf(sqrtf(rbf(vb + 1e-5f)));
    unsigned o[8];
#pragma unroll
    for (int i = 0; i < 8; ++i) {
        const float d0 = rbf(v[2 * i] - mb), d1 = rbf(v[2 * i + 1] - mb);
        o[i] = pk_bf16(d0 / den, d1 / den);
    }
    char* dst = (char*)xn + ((size_t)(row >> 5) * (D_ / 16) + lane) * 1024 + (row & 31) * 16;
    *(u32x4*)dst = (u32x4){o[0], o[1], o[2], o[3]};
    *(u32x4*)(dst + 512) = (u32x4){o[4], o[5], o[6], o[7]};
    TL_END(0);
}

constexpr size_t WS_XN = 0, WS_H = 4u << 20, WS_O = 20u << 20, WS_EXS = 36u << 20, WS_EXG = 40u << 20, WS_CNT = 44u << 20;

extern "C" void kernel_launch(void* const* d_in, const int* in_sizes, int n_in, void* d_out, int out_size, void* d_ws, size_t ws_size,
                              hipStream_t stream) {
    const u16* x = (const u16*)d_in[0];
    const u16* kc = (const u16*)d_in[1];
    const u16* vc = (const u16*)d_in[2];
    const u16* wq = (const u16*)d_in[3];
    const u16* wo = (const u16*)d_in[4];
    const u16* w1 = (const u16*)d_in[6];
    const u16* w2 = (const u16*)d_in[7];
    char* ws = (char*)d_ws;
    u16* xn = (u16*)(ws + WS_XN); u16* h = (u16*)(ws + WS_H); u16* o = (u16*)(ws + WS_O);
    unsigned* cnts = (unsigned*)(ws + WS_CNT);
    ln_kernel<<<256, 512, 0, stream>>>(x, xn, cnts);
    k2_kernel<<<256, 512, 0, stream>>>(xn, wq, kc, vc, w1, o, h, wo, w2);
    k3_kernel<<<256, 256, 0, stream>>>(o, wo, h, w2, x, (u16*)d_out, ws + WS_EXS, ws + WS_EXG, cnts);
}
extern "C" void launch_one(int which, void* const* d_in, void* d_out, void* d_ws, hipStream_t stream) {
    const u16* x = (const u16*)d_in[0];
    const u16* kc = (const u16*)d_in[1];
    const u16* vc = (const u16*)d_in[2];
    const u16* wq = (const u16*)d_in[3];
    const u16* wo = (const u16*)d_in[4];
    const u16* w1 = (const u16*)d_in[6];
    const u16* w2 = (const u16*)d_in[7];
    char* ws = (char*)d_ws;
    u16* xn = (u16*)(ws + WS_XN); u16* h = (u16*)(ws + WS_H); u16* o = (u16*)(ws + WS_O);
    unsigned* cnts = (unsigned*)(ws + WS_CNT);
    if (which == 0) ln_kernel<<<256, 512, 0, stream>>>(x, xn, cnts);
    if (which == 1) k2_kernel<<<256, 512, 0, stream>>>(xn, wq, kc, vc, w1, o, h, wo, w2);
    if (which == 2) { ln_kernel<<<1, 512, 0, stream>>>(x, xn, cnts); k3_kernel<<<256, 256, 0, stream>>>(o, wo, h, w2, x, (u16*)d_out, ws + WS_EXS, ws + WS_EXG, cnts); }
}
```

```cpp
#include <hip/hip_runtime.h>
#include <stdint.h>
#include <type_traits>

typedef short bf16x8 __attribute__((ext_vector_type(8)));
typedef short s16x4 __attribute__((ext_vector_type(4)));
typedef float f32x16 __attribute__((ext_vector_type(16)));
typedef float f32x4 __attribute__((ext_vector_type(4)));
typedef float f32x2 __attribute__((ext_vector_type(2)));
typedef unsigned u32x4 __attribute__((ext_vector_type(4)));
typedef unsigned u32x2 __attribute__((ext_vector_type(2)));
typedef unsigned short u16;
typedef __bf16 bf16x2_t __attribute__((ext_vector_type(2)));
#define LDSP __attribute__((address_space(3)))
#define DI __device__ __forceinline__

DI unsigned pk_bf16(float lo, float hi) {
    f32x2 f = {lo, hi};
    bf16x2_t b = __builtin_convertvector(f, bf16x2_t);
    return __builtin_bit_cast(unsigned, b);
}
DI float rbf(float x) {
    unsigned p = pk_bf16(x, x);
    return __uint_as_float(p << 16);
}
DI s16x4 tr_read(const char LDSP* p) { return __builtin_amdgcn_ds_read_tr16_b64_v4i16((s16x4 LDSP*)p); }
DI bf16x8 cat8(s16x4 lo, s16x4 hi) { return __builtin_shufflevector(lo, hi, 0, 1, 2, 3, 4, 5, 6, 7); }
#define MFMA32(a, b, c) __builtin_amdgcn_mfma_f32_32x32x16_bf16((a), (b), (c), 0, 0, 0)

template <int I, int N, class F>
DI void static_for(F&& f) {
    if constexpr (I < N) {
        f(std::integral_constant<int, I>{});
        static_for<I + 1, N>(f);
    }
}
template <int SH> struct AccT;
template <> struct AccT<32> { typedef f32x16 t; };
template <> struct AccT<16> { typedef f32x4 t; };

DI void wg_barrier() {
    asm volatile("" ::: "memory");
    __builtin_amdgcn_s_barrier();
    asm volatile("" ::: "memory");
}
DI void store_tile_frag_wt(__amdgpu_buffer_rsrc_t rs, unsigned base_off, const f32x16& a, int lane) {
    const unsigned p = base_off + (lane >> 5) * 1024 + (lane & 31) * 16;
    u32x4 w0, w1;
#pragma unroll
    for (int i = 0; i < 4; ++i) { w0[i] = pk_bf16(a[2 * i], a[2 * i + 1]); w1[i] = pk_bf16(a[8 + 2 * i], a[8 + 2 * i + 1]); }
    __builtin_amdgcn_raw_buffer_store_b128(w0, rs, p, 0, 16  );
    __builtin_amdgcn_raw_buffer_store_b128(w1, rs, p + 512, 0, 16  );
}

template <int BM_, int BN_, int NW_, int S_, int SH_>
struct DC {
    static constexpr int BM = BM_, BN = BN_, NW = NW_, S = S_, SH = SH_, BK = 64;
    static constexpr int NT = NW * 64;
    static constexpr int TT = SH, KK = SH == 32 ? 16 : 32;
    static constexpr int TW = BM / NW, TM = TW / TT, TN = BN / TT, KS = BK / KK, NTL = BN / 32;
    static constexpr int TM32 = TW / 32;
    static constexpr int WB = BN * BK * 2, WP = WB / 1024, LW = WP / NW;
    static constexpr int LB = TM * KS;
    static constexpr int LG = LW + LB;
    static constexpr int LDS_BYTES = S * WB;
    static constexpr int NM = TN * TM;
    static_assert(WP % NW == 0, "W pieces per wave");
    static_assert(BN % 128 == 0 && TW % 32 == 0, "shape");
    static_assert(SH == 32 || TN % 2 == 0, "TN even for the 16x16 shape");
    static_assert((S - 2) * LG <= 63, "vmcnt range");
};

template <class C>
struct DGState {
    const char* wbase;
    unsigned woff[C::LW];
    const char* xbase;
    unsigned xoff[C::TM32];
    long wstride;
    char LDSP* lds;
    unsigned lds_u;
    unsigned wdst;
    unsigned a_off[2];
};
template <class C>
struct DGFrags {
    s16x4 alo[2][C::TN], ahi[2][C::TN];
    bf16x8 b[C::S][C::KS][C::TM];
};

DI void glds16_asm(unsigned lds_addr, const char* sbase, unsigned voff) {
    asm volatile("s_mov_b32 m0, %0\n\ts_nop 0\n\tglobal_load_lds_dwordx4 %1, %2" :: "s"(lds_addr), "v"(voff), "s"(sbase) : "memory");
}
template <class C>
DI void dg_glds(const DGState<C>& st, int g, int t, int slot) {
    glds16_asm(st.lds_u + slot * C::WB + st.wdst + g * 1024, st.wbase + (long)t * st.wstride, st.woff[g]);
}
template <bool AG> DI void mfma_asm(f32x4& acc, const bf16x8& a, const bf16x8& b) {
    if constexpr (AG) asm volatile("v_mfma_f32_16x16x32_bf16 %0, %1, %2, %0" : "+a"(acc) : "v"(a), "v"(b));
    else asm volatile("v_mfma_f32_16x16x32_bf16 %0, %1, %2, %0" : "+v"(acc) : "v"(a), "v"(b));
}
template <bool AG> DI void mfma_asm(f32x16& acc, const bf16x8& a, const bf16x8& b) {
    if constexpr (AG) asm volatile("v_mfma_f32_32x32x16_bf16 %0, %1, %2, %0" : "+a"(acc) : "v"(a), "v"(b));
    else asm volatile("v_mfma_f32_32x32x16_bf16 %0, %1, %2, %0" : "+v"(acc) : "v"(a), "v"(b));
}
template <bool AG, class T, int TN, int TM>
DI void dg_zero_acc(T (&acc)[TN][TM]) {
#pragma unroll
    for (int a = 0; a < TN; ++a)
#pragma unroll
        for (int b = 0; b < TM; ++b) {
            acc[a][b] = (T){};
            if constexpr (AG) asm volatile("" : "+a"(acc[a][b]));
            else asm volatile("" : "+v"(acc[a][b]));
        }
    asm volatile("s_nop 1");
}
template <bool AG, class T, int TN, int TM>
DI void dg_drain_mfma(T (&acc)[TN][TM]) {
    asm volatile("s_nop 15");
#pragma unroll
    for (int a = 0; a < TN; ++a)
#pragma unroll
        for (int b = 0; b < TM; ++b) {
            if constexpr (AG) asm volatile("" : "+a"(acc[a][b]));
            else asm volatile("" : "+v"(acc[a][b]));
        }
    __builtin_amdgcn_sched_barrier(0);
}
template <int IMM>
DI void gload16_asm(bf16x8& dst, const char* sbase, unsigned voff) {
    asm volatile("global_load_dwordx4 %0, %1, %2 offset:%3" : "=v"(dst) : "v"(voff), "s"(sbase), "n"(IMM) : "memory");
}
template <class C, int BUF, int D>
DI void dg_bload(const DGState<C>& st, int t, DGFrags<C>& f) {
    constexpr int ks = D / C::TM, tb = D % C::TM;
    const char* sb = st.xbase + (long)t * 4096;
    if constexpr (C::SH == 32) gload16_asm<ks * 1024>(f.b[BUF][ks][tb], sb, st.xoff[tb]);
    else gload16_asm<(tb & 1) * 256 + ks * 2048>(f.b[BUF][ks][tb], sb, st.xoff[tb >> 1]);
}
template <class C>
DI void dg_aread(const DGState<C>& st, int k, int slot, int ks, int sel, DGFrags<C>& f) {
    const int ta = k >> 1, half = k & 1;
    const char LDSP* p;
    if constexpr (C::SH == 32) p = st.lds + (slot * C::WB + ((4 * ks + half) * C::NTL + ta) * 256) + st.a_off[0];
    else p = st.lds + (slot * C::WB + ((8 * ks + half) * C::NTL + (ta >> 1)) * 256) + st.a_off[ta & 1];
    if (half) f.ahi[sel][ta] = tr_read(p); else f.alo[sel][ta] = tr_read(p);
}
template <class C, int N0, int BUF>
DI void dg_wait(DGFrags<C>& f) {
    constexpr int N = N0;
    if constexpr (C::LB == 4) {
        constexpr int T = C::TM;
        asm volatile("s_waitcnt vmcnt(%4) lgkmcnt(0)"
                     : "+v"(f.b[BUF][0 / T][0 % T]), "+v"(f.b[BUF][1 / T][1 % T]), "+v"(f.b[BUF][2 / T][2 % T]), "+v"(f.b[BUF][3 / T][3 % T])
                     : "n"(N) : "memory");
    } else {
        static_assert(C::LB == 8, "LB");
        constexpr int T = C::TM;
        asm volatile("s_waitcnt vmcnt(%8) lgkmcnt(0)"
                     : "+v"(f.b[BUF][0 / T][0 % T]), "+v"(f.b[BUF][1 / T][1 % T]), "+v"(f.b[BUF][2 / T][2 % T]), "+v"(f.b[BUF][3 / T][3 % T]),
                       "+v"(f.b[BUF][4 / T][4 % T]), "+v"(f.b[BUF][5 / T][5 % T]), "+v"(f.b[BUF][6 / T][6 % T]), "+v"(f.b[BUF][7 / T][7 % T])
                     : "n"(N) : "memory");
    }
}
template <class C> constexpr int dg_aslot(int k) {
    return k * (C::NM - (C::NM / 4 > 1 ? C::NM / 4 : 1)) / (2 * C::TN);
}

template <class C, bool AG, bool TAIL, int B, int KSI, int EXTRA = 0>
DI void dg_step(const DGState<C>& st, int t, typename AccT<C::SH>::t (&acc)[C::TN][C::TM], DGFrags<C>& f) {
    constexpr int cur = (B * C::KS + KSI) & 1, nxt = cur ^ 1;
    constexpr bool LAST = KSI == C::KS - 1, FIRST = KSI == 0;
    constexpr bool HAS_NEXT = !(LAST && TAIL && B == C::S - 1);
    constexpr int nslot = LAST ? (B + 1) % C::S : B, nks = LAST ? 0 : KSI + 1;
    constexpr bool HAS_P = FIRST && (!TAIL || B == 0);
    constexpr int pslot = (B + C::S - 1) % C::S;
    if constexpr (LAST && HAS_NEXT) {
        dg_wait<C, (TAIL ? C::S - B - 2 : C::S - 2) * C::LG + EXTRA, (B + 1) % C::S>(f);
        wg_barrier();
    }
    static_for<0, C::NM>([&](auto ic) {
        constexpr int i = decltype(ic)::value, ta = i / C::TM, tb = i % C::TM;
        mfma_asm<AG>(acc[ta][tb], cat8(f.alo[cur][ta], f.ahi[cur][ta]), f.b[B][KSI][tb]);
        if constexpr (HAS_NEXT) {
            static_for<0, 2 * C::TN>([&](auto kc) {
                constexpr int k = decltype(kc)::value;
                if constexpr (dg_aslot<C>(k) == i) dg_aread<C>(st, k, nslot, nks, nxt, f);
            });
        }
        if constexpr (HAS_P) {
            static_for<0, C::LB>([&](auto dc) {
                constexpr int d = decltype(dc)::value;
                if constexpr (d * C::NM / C::LG == i) dg_bload<C, pslot, d>(st, t + C::S - 1, f);
            });
            static_for<0, C::LW>([&](auto gc) {
                constexpr int g = decltype(gc)::value;
                if constexpr ((C::LB + g) * C::NM / C::LG == i) dg_glds<C>(st, g, t + C::S - 1, pslot);
            });
        }
        __builtin_amdgcn_sched_barrier(0);
    });
}
template <class C, bool AG, bool TAIL, int B, int EXTRA = 0, int KSI = 0>
DI void dg_stage(const DGState<C>& st, int t, typename AccT<C::SH>::t (&acc)[C::TN][C::TM], DGFrags<C>& f) {
    dg_step<C, AG, TAIL, B, KSI, EXTRA>(st, t, acc, f);
    if constexpr (KSI + 1 < C::KS) dg_stage<C, AG, TAIL, B, EXTRA, KSI + 1>(st, t, acc, f);
}
template <class C, bool AG, bool TAIL, int B = 0>
DI void dg_group(const DGState<C>& st, int t0, typename AccT<C::SH>::t (&acc)[C::TN][C::TM], DGFrags<C>& f) {
    dg_stage<C, AG, TAIL, B>(st, t0 + B, acc, f);
    if constexpr (B + 1 < C::S) dg_group<C, AG, TAIL, B + 1>(st, t0, acc, f);
}

template <class C>
DI void dg_setup(DGState<C>& st, const char* Xw, int xks, const char* Wg, int ldw, char LDSP* lds) {
    const int tid = threadIdx.x, lane = tid & 63;
    const int wid = __builtin_amdgcn_readfirstlane(tid >> 6);
    st.lds = lds;
    const int q4 = (lane >> 2) & 3;
#pragma unroll
    for (int j = 0; j < C::LW; ++j) {
        const int pc = wid * C::LW + j, kq = pc / (C::BN / 128), a = pc % (C::BN / 128);
        const int cc = C::SH == 32 ? (lane & 3) : ((lane & 3) ^ (2 * ((kq >> 1) & 1)));
        st.woff[j] = (unsigned)(((4 * kq + q4) * ldw + 128 * a + 32 * (lane >> 4) + 8 * cc) * 2);
    }
    st.wbase = Wg;
    st.lds_u = (unsigned)(size_t)lds;
    st.wstride = (long)C::BK * ldw * 2;
    st.wdst = wid * C::LW * 1024;
    st.xbase = Xw;
    if constexpr (C::SH == 32) {
#pragma unroll
        for (int i = 0; i < C::TM32; ++i) st.xoff[i] = lane * 16 + i * xks * 1024;
        const int h = lane >> 5, gg = (lane >> 4) & 1, p = lane & 3, q4r = (lane & 15) >> 2;
        const int n4 = 4 * (p & 1) + 2 * gg + (p >> 1);
        st.a_off[0] = st.a_off[1] = (2 * h * C::NTL) * 256 + q4r * 64 + n4 * 8;
    } else {
        const int g = lane >> 4, p = lane & 3, q4r = (lane & 15) >> 2;
#pragma unroll
        for (int i = 0; i < C::TM32; ++i) st.xoff[i] = (g >> 1) * 1024 + (g & 1) * 512 + (lane & 15) * 16 + i * xks * 1024;
        const int base = (2 * g * C::NTL) * 256 + q4r * 64 + p * 8;
        st.a_off[0] = base + (g & 1) * 32;
        st.a_off[1] = base + ((g & 1) ^ 1) * 32;
    }
}
template <class C>
DI void dg_prologue(const DGState<C>& st, DGFrags<C>& f) {
    static_for<0, C::S - 1>([&](auto ic) {
        constexpr int i = decltype(ic)::value;
        static_for<0, C::LB>([&](auto dc) { dg_bload<C, i, decltype(dc)::value>(st, i, f); });
#pragma unroll
        for (int g = 0; g < C::LW; ++g) dg_glds<C>(st, g, i, i);
    });
}
template <class C, int EXTRA = 0>
DI void dg_start(const DGState<C>& st, DGFrags<C>& f) {
    dg_wait<C, (C::S - 2) * C::LG + EXTRA, 0>(f);
    wg_barrier();
    static_for<0, 2 * C::TN>([&](auto kc) { dg_aread<C>(st, decltype(kc)::value, 0, 0, 0, f); });
    __builtin_amdgcn_sched_barrier(0);
}
template <class C, bool AG>
DI void dg_run(const DGState<C>& st, int nt, typename AccT<C::SH>::t (&acc)[C::TN][C::TM], DGFrags<C>& f) {
    dg_start<C>(st, f);
    int t0 = 0;
    for (; t0 < nt - C::S; t0 += C::S) dg_group<C, AG, false>(st, t0, acc, f);
    dg_group<C, AG, true>(st, t0, acc, f);
    dg_drain_mfma<AG>(acc);
}

using K2Q = DC<256, 128, 8, 4, 32>;
using K2F = DC<256, 128, 8, 4, 16>;
#define K2_WARM 7
#define K2_WARM_A ((K2_WARM) & 1)
#define K2_WARM_B ((K2_WARM) & 2)
#define K2_WARM_C ((K2_WARM) & 4)
constexpr int K2_LDS = 160 * 1024;
constexpr int K2_TB = 16384;
DI constexpr int k2_kslot(int j) { return j % 3 == 0 ? 6 : (j % 3 == 1 ? 7 : 4); }
DI constexpr int k2_vslot(int j) { return (j & 3) == 0 ? 8 : ((j & 3) == 1 ? 9 : ((j & 3) == 2 ? 5 : 3)); }
constexpr float K2_C = 0.088388347648318440f * 1.4426950408889634f;

struct K2Attn {
    const char* kbase; const char* vbase;
    int wid;
    unsigned lds_u, dst;
    char LDSP* lds;
    unsigned k_addr, v_addrA, v_addrB;
};
DI void k2_issue_k(const K2Attn& a, int j) {
    int tz = threadIdx.x; asm volatile("" : "+v"(tz));
    const int lane = tz & 63;
#pragma unroll
    for (int i = 0; i < 2; ++i) {
        const int key = 4 * (a.wid * 2 + i) + (lane >> 4), c = (lane & 15) ^ (key & 15);
        glds16_asm(a.lds_u + k2_kslot(j) * K2_TB + a.dst + i * 1024, a.kbase + (long)j * K2_TB, (unsigned)(key * 256 + c * 16));
    }
}
DI void k2_issue_v(const K2Attn& a, int j) {
    int tz = threadIdx.x; asm volatile("" : "+v"(tz));
    const int lane = tz & 63;
#pragma unroll
    for (int i = 0; i < 2; ++i) {
        const int kv = 4 * (a.wid * 2 + i) + ((lane >> 2) & 3);
        glds16_asm(a.lds_u + k2_vslot(j) * K2_TB + a.dst + i * 1024, a.vbase + (long)j * K2_TB, (unsigned)(kv * 256 + ((lane >> 4) * 32 + (lane & 3) * 8) * 2));
    }
}
template <int J, int I>
DI bf16x8 k2_kfrag(const K2Attn& a) {
    constexpr int u = I >> 1, kt = I & 1, cu = 4 * (u >> 1) + (u & 1);
    return *(const bf16x8 LDSP*)(a.lds + ((a.k_addr ^ (unsigned)(cu << 4)) + (unsigned)((k2_kslot(J) - 4) * K2_TB + kt * 8192)));
}
template <int J, int I, int HALF>
DI s16x4 k2_vfrag(const K2Attn& a) {
    constexpr int t = I & 3, ks = I >> 2, sl = k2_vslot(J);
    constexpr int off = ((4 * ks + 2 * HALF) * 4 + t) * 256;
    if constexpr (sl == 3 || sl == 5) return tr_read(a.lds + ((sl - 3) * K2_TB + off) + a.v_addrB);
    else return tr_read(a.lds + ((sl - 8) * K2_TB + off) + a.v_addrA);
}
template <int N> DI void wait_vm() { asm volatile("s_waitcnt vmcnt(%0)" ::"n"(N) : "memory"); }

constexpr float K2_THR = 6.0f / K2_C;
struct K2Soft {
    f32x16 o[4];
    float m_run, l_run, alpha, mc, ps, tm;
    bool resc;
    u32x4 pf[2][4];
    f32x16 T[3];
    bf16x8 kfr[4];
    s16x4 vlo[4], vhi[4];
};
DI float max3_asm(float a, float b, float c) { float r; asm volatile("v_max3_f32 %0, %1, %2, %3" : "=v"(r) : "v"(a), "v"(b), "v"(c)); return r; }
template <class T> DI void anchor(T& x) { asm volatile("" : "+v"(x)); }
template <int PAR, int F, int W>
DI void k2_exp4(K2Soft& z, float s0, float s1, float s2, float s3) {
    const float p0 = __builtin_amdgcn_exp2f(fmaf(s0, K2_C, z.mc)), p1 = __builtin_amdgcn_exp2f(fmaf(s1, K2_C, z.mc));
    const float p2 = __builtin_amdgcn_exp2f(fmaf(s2, K2_C, z.mc)), p3 = __builtin_amdgcn_exp2f(fmaf(s3, K2_C, z.mc));
    z.ps += (p0 + p1) + (p2 + p3);
    unsigned k0 = pk_bf16(p0, p1), k1 = pk_bf16(p2, p3);
    anchor(z.ps); anchor(k0); anchor(k1);
    z.pf[PAR][F][W] = k0;
    z.pf[PAR][F][W + 1] = k1;
}
template <int j, int c>
DI void k2_sm_start(K2Soft& z) {
    f32x16& sa = z.T[(2 * j) % 3]; f32x16& sb = z.T[(2 * j + 1) % 3];
    if constexpr (c < 4) {
        float t = c == 0 ? max3_asm(sa[0], sa[1], sa[2]) : max3_asm(z.tm, sa[4 * c], sa[4 * c + 1]);
        t = c == 0 ? max3_asm(t, sa[3], sa[3]) : max3_asm(t, sa[4 * c + 2], sa[4 * c + 3]);
        t = max3_asm(t, sb[4 * c], sb[4 * c + 1]);
        z.tm = max3_asm(t, sb[4 * c + 2], sb[4 * c + 3]);
    } else if constexpr (c == 4) {
        auto rr = __builtin_amdgcn_permlane32_swap(__float_as_uint(z.tm), __float_as_uint(z.tm), false, false);
        const float tmax = fmaxf(__uint_as_float(rr[0]), __uint_as_float(rr[1]));
        z.resc = __any(tmax - z.m_run > K2_THR);
        const float m_new = z.resc ? fmaxf(z.m_run, tmax) : z.m_run;
        z.alpha = __builtin_amdgcn_exp2f((z.m_run - m_new) * K2_C);
        z.m_run = m_new;
        z.mc = -m_new * K2_C;
        z.ps = 0.f;
        anchor(z.alpha); anchor(z.mc);
    } else if constexpr (c == 5 || c == 7 || c == 9 || c == 11) {
        constexpr int k = (c - 5) / 2;
        k2_exp4<j & 1, (k >> 1), 2 * (k & 1)>(z, sa[4 * k], sa[4 * k + 1], sa[4 * k + 2], sa[4 * k + 3]);
    }
}
template <int j, int c>
DI void k2_sm_finish(K2Soft& z) {
    f32x16& sb = z.T[(2 * j + 1) % 3];
    if constexpr (c == 0 || c == 2 || c == 4 || c == 6) {
        constexpr int k = c / 2;
        k2_exp4<j & 1, 2 + (k >> 1), 2 * (k & 1)>(z, sb[4 * k], sb[4 * k + 1], sb[4 * k + 2], sb[4 * k + 3]);
    } else if constexpr (c == 8) {
        z.l_run = z.l_run * z.alpha + z.ps;
        anchor(z.l_run);
    }
}
template <int j>
DI void k2_phaseA(const K2Attn& a, const bf16x8 (&qf)[8], K2Soft& z) {
    constexpr int PFK = 3;
    f32x16& sa = z.T[(2 * j) % 3]; f32x16& sb = z.T[(2 * j + 1) % 3];
    if constexpr (j < 8) {
        static_for<0, PFK>([&](auto ic) { constexpr int i = decltype(ic)::value; z.kfr[i & 3] = k2_kfrag<(j < 8 ? j : 0), i>(a); });
        __builtin_amdgcn_sched_barrier(0);
    }
    if constexpr (j >= 2) {
        if (z.resc) {
#pragma unroll
            for (int t = 0; t < 4; ++t)
#pragma unroll
                for (int r = 0; r < 16; ++r) z.o[t][r] *= z.alpha;
        }
        __builtin_amdgcn_sched_barrier(0);
    }
    static_for<0, 16>([&](auto ic) {
        constexpr int i = decltype(ic)::value;
        if constexpr (j < 8) {
            if constexpr (i == 0) sa = MFMA32(z.kfr[0], qf[0], (f32x16){});
            else if constexpr (i == 1) sb = MFMA32(z.kfr[1], qf[0], (f32x16){});
            else if constexpr (i & 1) sb = MFMA32(z.kfr[i & 3], qf[i >> 1], sb);
            else sa = MFMA32(z.kfr[i & 3], qf[i >> 1], sa);
            if constexpr (i + PFK < 16) z.kfr[(i + PFK) & 3] = k2_kfrag<(j < 8 ? j : 0), (i + PFK < 16 ? i + PFK : 0)>(a);
        }
        if constexpr (j >= 1) k2_sm_finish<j - 1, i>(z);
        __builtin_amdgcn_sched_barrier(0);
    });
}
template <int j>
DI void k2_phaseB(const K2Attn& a, K2Soft& z) {
    constexpr int PFV = 2;
    if constexpr (j >= 1) {
        static_for<0, PFV>([&](auto ic) { constexpr int i = decltype(ic)::value;
            z.vlo[i & 3] = k2_vfrag<(j >= 1 ? j - 1 : 0), i, 0>(a); z.vhi[i & 3] = k2_vfrag<(j >= 1 ? j - 1 : 0), i, 1>(a); });
    }
    if constexpr (j < 8) asm volatile("s_nop 11");
    __builtin_amdgcn_sched_barrier(0);
    static_for<0, 16>([&](auto ic) {
        constexpr int i = decltype(ic)::value;
        if constexpr (j >= 1) {
            z.o[i & 3] = MFMA32(cat8(z.vlo[i & 3], z.vhi[i & 3]), __builtin_bit_cast(bf16x8, z.pf[(j - 1) & 1][i >> 2]), z.o[i & 3]);
            if constexpr (i + PFV < 16) {
                z.vlo[(i + PFV) & 3] = k2_vfrag<(j >= 1 ? j - 1 : 0), (i + PFV < 16 ? i + PFV : 0), 0>(a);
                z.vhi[(i + PFV) & 3] = k2_vfrag<(j >= 1 ? j - 1 : 0), (i + PFV < 16 ? i + PFV : 0), 1>(a);
            }
        }
        if constexpr (j < 8) k2_sm_start<(j < 8 ? j : 0), i>(z);
        __builtin_amdgcn_sched_barrier(0);
    });
}

constexpr int QF_S = 3, QF_WB = 32768, QF_PF = 4, QF_R = 6, QF_RB = 6, QF_PB = 5, QF_NT = 16;
struct QFState {
    const char* bq; const char* bf;
    unsigned woff[4];
    const char* xbase; unsigned xoff;
    unsigned lds_u, wdst, a_off; char LDSP* lds;
};
struct QFFrags { s16x4 alo[QF_R], ahi[QF_R]; bf16x8 b[QF_RB]; };
constexpr int qf_hook(int T) { return T <= 3 ? 2 : 0; }
constexpr int qf_younger(int type, int idx, int stopT, int stopN, int warm) {
    int cnt = 0; bool seen = false;
    for (int g = 0; g < 4; ++g) { if (seen) ++cnt; }
    if (type == 0 && idx == 0) seen = true;
    for (int q = 0; q < QF_PB; ++q) { if (seen) ++cnt; if (type == 1 && idx == q) seen = true; }
    for (int g = 0; g < 4; ++g) { if (seen) ++cnt; }
    if (type == 0 && idx == 1) seen = true;
    for (int T = 0; T < QF_NT; ++T) {
        for (int n = 0; n < 32; ++n) {
            if (T == stopT && n == stopN) return cnt;
            if (n % 8 == 0 && 4 * T + n / 8 + QF_PB < 4 * QF_NT) { if (seen) ++cnt; if (type == 1 && idx == 4 * T + n / 8 + QF_PB) seen = true; }
            if (n >= 1 && n <= 4 && T + 2 < QF_NT) { if (seen) ++cnt; if (n == 4 && type == 0 && idx == T + 2) seen = true; }
        }
        for (int h = 0; h < qf_hook(T) + (T == 0 ? warm : 0); ++h) { if (seen) ++cnt; }
    }
    return cnt;
}
DI void qf_glds(const QFState& st, int g, int t) {
    glds16_asm(st.lds_u + (t % QF_S) * QF_WB + st.wdst + g * 1024, (g & 1) ? st.bf + (long)t * (64 * 7168) : st.bq + (long)t * (64 * 8192), st.woff[g]);
}
template <int Q> DI void qf_bload(const QFState& st, QFFrags& f) { gload16_asm<(Q & 3) * 1024>(f.b[Q % QF_RB], st.xbase + (long)(Q >> 2) * 4096, st.xoff); }
template <int T, int M> DI void qf_aread(const QFState& st, QFFrags& f) {
    constexpr int ks = M >> 3, ta = M & 7, r = (32 * T + M) % QF_R;
    const char LDSP* p = st.lds + ((T % QF_S) * QF_WB + ((4 * ks) * 8 + ta) * 256) + st.a_off;
    f.alo[r] = tr_read(p);
    f.ahi[r] = tr_read(p + 8 * 256);
}
template <int T, int WARM>
DI void qf_stage(const QFState& st, f32x16 (&acc)[8][1], QFFrags& f) {
    static_for<0, 32>([&](auto nc) {
        constexpr int n = decltype(nc)::value, q = 4 * T + (n >> 3), r = (32 * T + n) % QF_R;
        if constexpr (n % 8 == 0) {
            asm volatile("s_waitcnt vmcnt(%1)" : "+v"(f.b[q % QF_RB]) : "n"(qf_younger(1, q, T, n, WARM)) : "memory");
        }
        mfma_asm<false>(acc[n & 7][0], cat8(f.alo[r], f.ahi[r]), f.b[q % QF_RB]);
        if constexpr (n + QF_PF < 32) qf_aread<T, (n + QF_PF < 32 ? n + QF_PF : 0)>(st, f);
        else if constexpr (T + 1 < QF_NT) qf_aread<T + 1, (n + QF_PF - 32)>(st, f);
        if constexpr (n % 8 == 0 && q + QF_PB < 4 * QF_NT) qf_bload<(q + QF_PB < 4 * QF_NT ? q + QF_PB : 0)>(st, f);
        if constexpr (n >= 1 && n <= 4 && T + 2 < QF_NT) qf_glds(st, n - 1, T + 2);
        if constexpr (n == 31 - QF_PF && T + 1 < QF_NT) {
            asm volatile("s_waitcnt vmcnt(%0) lgkmcnt(0)" :: "n"(qf_younger(0, T + 1, T, n + 1, WARM)) : "memory");
            wg_barrier();
        }
        __builtin_amdgcn_sched_barrier(0);
    });
}

__global__ __launch_bounds__(512, 2) void k2_kernel(const u16* __restrict__ xn, const u16* __restrict__ wq, const u16* __restrict__ kc, const u16* __restrict__ vc,
                                                    const u16* __restrict__ w1, u16* __restrict__ o_out, u16* __restrict__ h_out,
                                                    const u16* __restrict__ wo, const u16* __restrict__ w2
                                                    ) {
    __shared__ __attribute__((aligned(1024))) char smem[K2_LDS];
    const int bid = blockIdx.x;
    const int head = (bid & 7) * 4 + ((bid >> 3) & 3), qb = bid >> 5;
    const int wid = __builtin_amdgcn_readfirstlane(threadIdx.x >> 6);
    char LDSP* lds = (char LDSP*)smem;
    const char* Xw = (const char*)xn + (size_t)(qb * 8 + wid) * 64 * 1024;
    const bool hasF = head < 28;
    K2Attn a;
    {
        const int kvh = head >> 3;
        a.kbase = (const char*)(kc + ((size_t)kvh * 2048 + 1536) * 128);
        a.vbase = (const char*)(vc + ((size_t)kvh * 2048 + 1536) * 128);
        a.lds = lds; a.lds_u = (unsigned)(size_t)lds; a.dst = wid * 2048; a.wid = wid;
    }
    bf16x8 qf[8];
    auto pack_q = [&](auto& acc) {
#pragma unroll
        for (int u = 0; u < 8; ++u) {
            u32x4 w;
#pragma unroll
            for (int i = 0; i < 4; ++i) w[i] = pk_bf16(acc[u >> 1][0][8 * (u & 1) + 2 * i], acc[u >> 1][0][8 * (u & 1) + 2 * i + 1]);
            qf[u] = __builtin_bit_cast(bf16x8, w);
        }
    };
    if (hasF) {
        f32x16 acc[8][1];
        dg_zero_acc<false>(acc);
        QFState st; QFFrags f;
        {
            const int lane = threadIdx.x & 63, q4 = (lane >> 2) & 3;
            st.bq = (const char*)(wq + head * 128); st.bf = (const char*)(w1 + head * 128);
#pragma unroll
            for (int g = 0; g < 4; ++g)
                st.woff[g] = (unsigned)(((4 * (2 * wid + (g >> 1)) + q4) * ((g & 1) ? 3584 : 4096) + 32 * (lane >> 4) + 8 * (lane & 3)) * 2);
            st.xbase = Xw; st.xoff = lane * 16;
            st.lds = lds; st.lds_u = (unsigned)(size_t)lds; st.wdst = wid * 4096;
            const int h = lane >> 5, gg = (lane >> 4) & 1, p = lane & 3, q4r = (lane & 15) >> 2;
            st.a_off = (2 * h * 8) * 256 + q4r * 64 + (4 * (p & 1) + 2 * gg + (p >> 1)) * 8;
        }
#pragma unroll
        for (int g = 0; g < 4; ++g) qf_glds(st, g, 0);
        static_for<0, QF_PB>([&](auto qc) { qf_bload<decltype(qc)::value>(st, f); });
#pragma unroll
        for (int g = 0; g < 4; ++g) qf_glds(st, g, 1);
        constexpr int WARM = K2_WARM_A ? 8 : 0;
        asm volatile("s_waitcnt vmcnt(%0)" :: "n"(qf_younger(0, 0, 0, 0, WARM)) : "memory");
        wg_barrier();
        static_for<0, QF_PF>([&](auto mc) { qf_aread<0, decltype(mc)::value>(st, f); });
        __builtin_amdgcn_sched_barrier(0);
        static_for<0, 16>([&](auto tc) {
            constexpr int t = decltype(tc)::value;
            qf_stage<t, WARM>(st, acc, f);
            if constexpr (t == 0) k2_issue_k(a, 0);
            if constexpr (t == 0) {
#pragma unroll
            for (int u = 0; u < 2; ++u) {
                const int sg = 2 + 2 * qb + u < 15 ? 2 + 2 * qb + u : 15;
#pragma unroll
                for (int g = 0; g < 4; ++g)
                    glds16_asm(st.lds_u + 9 * K2_TB + a.dst + (g & 1) * 1024, (g & 1) ? st.bf + (long)sg * (64 * 7168) : st.bq + (long)sg * (64 * 8192), st.woff[g]);
            }
            }

            if constexpr (t == 1) k2_issue_v(a, 0);
            if constexpr (t == 2) k2_issue_k(a, 1);
            if constexpr (t == 3) k2_issue_v(a, 1);
        });
        dg_drain_mfma<false>(acc);
        {
            int tz = threadIdx.x; asm volatile("" : "+v"(tz));
            const __amdgpu_buffer_rsrc_t hrs = __builtin_amdgcn_make_buffer_rsrc(h_out, 0, 2048u * 3584u * 2u, 0x00020000);
            const unsigned hbase = (unsigned)(((qb * 8 + wid) * 224 + head * 8) * 1024);
#pragma unroll
            for (int t = 0; t < 4; ++t) {
#pragma unroll
                for (int r = 0; r < 16; ++r) acc[4 + t][0][r] = fmaxf(acc[4 + t][0][r], 0.f);
                store_tile_frag_wt(hrs, hbase + t * 2048, acc[4 + t][0], tz & 63);
            }
        }
        pack_q(acc);
    } else {
        f32x16 accq[4][1];
        dg_zero_acc<false>(accq);
        DGState<K2Q> st; DGFrags<K2Q> f;
        dg_setup<K2Q>(st, Xw, 64, (const char*)(wq + head * 128), 4096, lds);
        dg_prologue<K2Q>(st, f);
        dg_start<K2Q>(st, f);
        static_for<0, 16>([&](auto tc) {
            constexpr int t = decltype(tc)::value;
            constexpr auto hook = [](int u) { return (u >= 0 && u <= 3) ? 2 : 0; };
            dg_stage<K2Q, false, (t >= 12), (t & 3), hook(t - 1) + hook(t - 2)>(st, t, accq, f);
            if constexpr (t == 0) k2_issue_k(a, 0);
            if constexpr (t == 1) k2_issue_v(a, 0);
            if constexpr (t == 2) k2_issue_k(a, 1);
            if constexpr (t == 3) k2_issue_v(a, 1);
        });
        dg_drain_mfma<false>(accq);
        pack_q(accq);
    }
    {
        int tz = threadIdx.x; asm volatile("" : "+v"(tz));
        const int lane = tz & 63, h = lane >> 5;
        a.k_addr = 4 * K2_TB + (lane & 31) * 256 + ((((2 * h) ^ (lane & 15)) & 15) << 4);
        const int gg = (lane >> 4) & 1, p = lane & 3, q4 = (lane & 15) >> 2;
        const int n4 = 4 * (p & 1) + 2 * gg + (p >> 1);
        const unsigned vl = h * 4 * 256 + q4 * 64 + n4 * 8;
        a.v_addrA = 8 * K2_TB + vl; a.v_addrB = 3 * K2_TB + vl;
        asm volatile("" : "+v"(a.k_addr), "+v"(a.v_addrA), "+v"(a.v_addrB));
    }
    K2Soft z;
#pragma unroll
    for (int t = 0; t < 4; ++t) z.o[t] = (f32x16){};
    z.m_run = -INFINITY; z.l_run = 0.f; z.alpha = 1.f; z.mc = 0.f; z.ps = 0.f; z.resc = false;
    auto top = [&](auto ic) {
        constexpr int i = decltype(ic)::value;
        if constexpr (i >= 2) wait_vm<(i - 1 <= 5 ? 4 : (i - 1 == 6 ? 2 : 0))>();
        wg_barrier();
        if constexpr (i <= 5) k2_issue_k(a, i + 2);
        if constexpr (i >= 1 && i <= 6) k2_issue_v(a, i + 1);
        if constexpr (i < 8) {
            int pc = (bid * 8 + wid) * 8 + i;
            pc = pc < 15360 ? pc : 15359;
            const char* src = pc < 8192 ? (const char*)wo + (size_t)pc * 1024 : (const char*)w2 + (size_t)(pc - 8192) * 1024;
            int tz = threadIdx.x; asm volatile("" : "+v"(tz));
            glds16_asm(a.lds_u + K2_TB + wid * 1024, src, (unsigned)(tz & 63) * 16);
        }
    };
    if (wid < 4) {
        top(std::integral_constant<int, 0>{});
        static_for<0, 9>([&](auto jc) {
            constexpr int j = decltype(jc)::value;
            if constexpr (j >= 1) top(jc);
            k2_phaseA<j>(a, qf, z);
            k2_phaseB<j>(a, z);
        });
    } else {
        top(std::integral_constant<int, 0>{});
        static_for<0, 9>([&](auto jc) {
            constexpr int j = decltype(jc)::value;
            k2_phaseA<j>(a, qf, z);
            if constexpr (j < 8) top(std::integral_constant<int, j + 1>{});
            k2_phaseB<j>(a, z);
        });
    }
    {
        int tz = threadIdx.x; asm volatile("" : "+v"(tz));
        const int lane = tz & 63;
        auto rr = __builtin_amdgcn_permlane32_swap(__float_as_uint(z.l_run), __float_as_uint(z.l_run), false, false);
        const float inv = 1.0f / (__uint_as_float(rr[0]) + __uint_as_float(rr[1]));
        const __amdgpu_buffer_rsrc_t ors = __builtin_amdgcn_make_buffer_rsrc(o_out, 0, 2048u * 4096u * 2u, 0x00020000);
        const unsigned obase = (unsigned)(((qb * 8 + wid) * 256 + head * 8) * 1024);
#pragma unroll
        for (int t = 0; t < 4; ++t) {
#pragma unroll
            for (int r = 0; r < 16; ++r) z.o[t][r] *= inv;
            store_tile_frag_wt(ors, obase + t * 2048, z.o[t], lane);
        }
    }
}

using K3C = DC<128, 128, 4, 4, 16>;
constexpr int K3_STG_PITCH = 272;
constexpr int K3_STG_WAVE = 32 * K3_STG_PITCH;
constexpr int K3_LDS = K3C::LDS_BYTES + 4 * K3_STG_WAVE + 16;

typedef __attribute__((address_space(1))) unsigned gu32;
DI unsigned atomic_load_agent(unsigned* p) { return __hip_atomic_load(p, __ATOMIC_RELAXED, __HIP_MEMORY_SCOPE_AGENT); }
DI unsigned atomic_add_agent(unsigned* p, unsigned v) { return __hip_atomic_fetch_add(p, v, __ATOMIC_RELAXED, __HIP_MEMORY_SCOPE_AGENT); }

DI float silu_bf(float a) {
    const float x = rbf(a);
    const float e = __builtin_amdgcn_exp2f(-x * 1.4426950408889634f);
    return rbf(x * __builtin_amdgcn_rcpf(1.0f + e));
}

DI bool k3_exchange(const u32x4 (&mine)[8], u32x4 (&theirs)[8], unsigned c0, unsigned* cnt, char* my_slot, char* their_slot, int lane, int wid, volatile unsigned LDSP* word) {
    if (wid == 0) {
        if (lane == 0) *word = c0;
    }
    __syncthreads();
    unsigned c = __builtin_amdgcn_readfirstlane(*word);
    if (c == 0) {
        const __amdgpu_buffer_rsrc_t rs = __builtin_amdgcn_make_buffer_rsrc(my_slot, 0, 8192u, 0x00020000);
#pragma unroll
        for (int i = 0; i < 8; ++i) __builtin_amdgcn_raw_buffer_store_b128(mine[i], rs, i * 1024 + lane * 16, 0, 16  );
        asm volatile("s_waitcnt vmcnt(0)" ::: "memory");
        __syncthreads();
        if (wid == 0) {
            unsigned old = 0;
            if (lane == 0) old = atomic_add_agent(cnt, 1u);
            if (lane == 0) *word = old;
        }
        __syncthreads();
        c = __builtin_amdgcn_readfirstlane(*word);
        if (c == 0) return false;
    }
    const __amdgpu_buffer_rsrc_t rt = __builtin_amdgcn_make_buffer_rsrc(their_slot, 0, 8192u, 0x00020000);
#pragma unroll
    for (int i = 0; i < 8; ++i) theirs[i] = __builtin_amdgcn_raw_buffer_load_b128(rt, i * 1024 + lane * 16, 0, 16  );
    return true;
}

__global__ __launch_bounds__(256, 1) void k3_kernel(const u16* __restrict__ o_f, const u16* __restrict__ wo, const u16* __restrict__ h_f,
                                                    const u16* __restrict__ w2, const u16* __restrict__ x, u16* __restrict__ out,
                                                    char* __restrict__ exs, char* __restrict__ exg, unsigned* __restrict__ cnts
                                                    ) {
    using C = K3C;
    __shared__ __attribute__((aligned(1024))) char smem[K3_LDS];
    const int bid = blockIdx.x, xcd = bid & 7, i5 = bid >> 3;
    const int type = xcd >> 2, pm = (xcd & 3) * 4 + (i5 & 3), pn = i5 >> 2;
    const int tile = pm * 8 + pn;
    const int wid = __builtin_amdgcn_readfirstlane(threadIdx.x >> 6);
    f32x4 acc[C::TN][C::TM];
    dg_zero_acc<true>(acc);
    const int Kd = type == 0 ? 4096 : 3584;
    {
        const char* Xw = (const char*)(type == 0 ? o_f : h_f) + (size_t)(pm * 4 + wid) * (Kd / 16) * 1024;
        const u16* W = (type == 0 ? wo : w2) + pn * 128;
        DGState<C> st; DGFrags<C> f;
        dg_setup<C>(st, Xw, Kd / 16, (const char*)W, 1024, (char LDSP*)smem);
        dg_prologue<C>(st, f);
        dg_run<C, true>(st, Kd / 64, acc, f);
    }
    int tz = threadIdx.x; asm volatile("" : "+v"(tz));
    const int lane = tz & 63;
    unsigned c0 = 0;
    if (wid == 0) c0 = atomic_load_agent(cnts + tile * 4);
    const size_t row0 = (size_t)pm * 128 + wid * 32;
    const u16* xp = x + (row0 + (lane >> 4)) * 1024 + pn * 128 + (lane & 15) * 8;
    u32x4 xv[8];
#pragma unroll
    for (int i = 0; i < 8; ++i) xv[i] = *(const u32x4*)(xp + (size_t)i * 4 * 1024);
    u32x4 mine[8], theirs[8];
#pragma unroll
    for (int i = 0; i < 8; ++i) {
        if (type == 0) mine[i] = (u32x4){pk_bf16(acc[i][0][0], acc[i][0][1]), pk_bf16(acc[i][0][2], acc[i][0][3]), pk_bf16(acc[i][1][0], acc[i][1][1]), pk_bf16(acc[i][1][2], acc[i][1][3])};
        else mine[i] = (u32x4){pk_bf16(silu_bf(acc[i][0][0]), silu_bf(acc[i][0][1])), pk_bf16(silu_bf(acc[i][0][2]), silu_bf(acc[i][0][3])),
                               pk_bf16(silu_bf(acc[i][1][0]), silu_bf(acc[i][1][1])), pk_bf16(silu_bf(acc[i][1][2]), silu_bf(acc[i][1][3]))};
    }
    const size_t slot = ((size_t)tile * 4 + wid) * 8192;
    if (!k3_exchange(mine, theirs, c0, cnts + tile * 4, (type == 0 ? exs : exg) + slot, (type == 0 ? exg : exs) + slot, lane, wid,
                     (volatile unsigned LDSP*)((char LDSP*)smem + K3_LDS - 16))) {
        return;
    }
    char LDSP* stg = (char LDSP*)smem + C::LDS_BYTES + wid * K3_STG_WAVE;
#pragma unroll
    for (int i = 0; i < 8; ++i)
#pragma unroll
        for (int tb = 0; tb < 2; ++tb) {
            const unsigned a0 = mine[i][2 * tb], a1 = mine[i][2 * tb + 1], b0 = theirs[i][2 * tb], b1 = theirs[i][2 * tb + 1];
            const float p0 = __uint_as_float(a0 << 16) * __uint_as_float(b0 << 16), p1 = __uint_as_float(a0 & 0xffff0000u) * __uint_as_float(b0 & 0xffff0000u);
            const float p2 = __uint_as_float(a1 << 16) * __uint_as_float(b1 << 16), p3 = __uint_as_float(a1 & 0xffff0000u) * __uint_as_float(b1 & 0xffff0000u);
            *(u32x2 LDSP*)(stg + (16 * tb + (lane & 15)) * K3_STG_PITCH + (16 * i + 4 * (lane >> 4)) * 2) = (u32x2){pk_bf16(p0, p1), pk_bf16(p2, p3)};
        }
    u16* op = out + (row0 + (lane >> 4)) * 1024 + pn * 128 + (lane & 15) * 8;
#pragma unroll
    for (int i = 0; i < 8; ++i) {
        const u32x4 pv = *(const u32x4 LDSP*)(stg + (4 * i + (lane >> 4)) * K3_STG_PITCH + (lane & 15) * 16);
        u32x4 ov;
#pragma unroll
        for (int e = 0; e < 4; ++e)
            ov[e] = pk_bf16(__uint_as_float(xv[i][e] << 16) + __uint_as_float(pv[e] << 16), __uint_as_float(xv[i][e] & 0xffff0000u) + __uint_as_float(pv[e] & 0xffff0000u));
        *(u32x4*)(op + (size_t)i * 4 * 1024) = ov;
    }
}

constexpr int M_ = 2048, D_ = 1024;

DI float wave_sum(float v) {
    int x = __float_as_int(v);
#define DPP_ADD(ctrl, rmask) x = __float_as_int(__int_as_float(x) + __int_as_float(__builtin_amdgcn_update_dpp(0, x, ctrl, rmask, 0xf, true)))
    DPP_ADD(0x111, 0xf); DPP_ADD(0x112, 0xf); DPP_ADD(0x114, 0xf); DPP_ADD(0x118, 0xf);
    DPP_ADD(0x142, 0xa); DPP_ADD(0x143, 0xc);
#undef DPP_ADD
    return __int_as_float(__builtin_amdgcn_readlane(x, 63));
}
__global__ __launch_bounds__(512) void ln_kernel(const u16* __restrict__ x, u16* __restrict__ xn, unsigned* __restrict__ cnts) {
    const int lane = threadIdx.x & 63, wid = threadIdx.x >> 6;
    if (blockIdx.x == 0 && threadIdx.x < 128) cnts[threadIdx.x * 4] = 0u;
    const int row = blockIdx.x * 8 + wid;
    const u32x4* src = (const u32x4*)(x + (size_t)row * D_ + lane * 16);
    u32x4 a = src[0], b = src[1];
    float v[16];
#pragma unroll
    for (int i = 0; i < 4; ++i) { v[2 * i] = __uint_as_float(a[i] << 16); v[2 * i + 1] = __uint_as_float(a[i] & 0xffff0000u);
                                  v[8 + 2 * i] = __uint_as_float(b[i] << 16); v[8 + 2 * i + 1] = __uint_as_float(b[i] & 0xffff0000u); }
    float s = 0.f;
#pragma unroll
    for (int i = 0; i < 16; ++i) s += v[i];
    const float mean = wave_sum(s) * (1.0f / 1024.0f);
    float q = 0.f;
#pragma unroll
    for (int i = 0; i < 16; ++i) { const float d = v[i] - mean; q += d * d; }
    const float var = wave_sum(q) * (1.0f / 1024.0f);
    const float mb = rbf(mean), vb = rbf(var);
    const float den = rbf(sqrtf(rbf(vb + 1e-5f)));
    unsigned o[8];
#pragma unroll
    for (int i = 0; i < 8; ++i) {
        const float d0 = rbf(v[2 * i] - mb), d1 = rbf(v[2 * i + 1] - mb);
        o[i] = pk_bf16(d0 / den, d1 / den);
    }
    char* dst = (char*)xn + ((size_t)(row >> 5) * (D_ / 16) + lane) * 1024 + (row & 31) * 16;
    *(u32x4*)dst = (u32x4){o[0], o[1], o[2], o[3]};
    *(u32x4*)(dst + 512) = (u32x4){o[4], o[5], o[6], o[7]};
}

constexpr size_t WS_XN = 0, WS_H = 4u << 20, WS_O = 20u << 20, WS_EXS = 36u << 20, WS_EXG = 40u << 20, WS_CNT = 44u << 20;

extern "C" void kernel_launch(void* const* d_in, const int* in_sizes, int n_in, void* d_out, int out_size, void* d_ws, size_t ws_size,
                              hipStream_t stream) {
    const u16* x = (const u16*)d_in[0];
    const u16* kc = (const u16*)d_in[1];
    const u16* vc = (const u16*)d_in[2];
    const u16* wq = (const u16*)d_in[3];
    const u16* wo = (const u16*)d_in[4];
    const u16* w1 = (const u16*)d_in[6];
    const u16* w2 = (const u16*)d_in[7];
    char* ws = (char*)d_ws;
    u16* xn = (u16*)(ws + WS_XN); u16* h = (u16*)(ws + WS_H); u16* o = (u16*)(ws + WS_O);
    unsigned* cnts = (unsigned*)(ws + WS_CNT);
    ln_kernel<<<256, 512, 0, stream>>>(x, xn, cnts);
    k2_kernel<<<256, 512, 0, stream>>>(xn, wq, kc, vc, w1, o, h, wo, w2);
    k3_kernel<<<256, 256, 0, stream>>>(o, wo, h, w2, x, (u16*)d_out, ws + WS_EXS, ws + WS_EXG, cnts);
}
```
